```python
import torch

def _softmax(x, axis=-1):
    return torch.softmax(x, dim=axis)
def _gelu(x):
    return torch.nn.functional.gelu(x, approximate="tanh")
def _silu(x):
    return torch.nn.functional.silu(x)
def _sigmoid(x):
    return torch.sigmoid(x)
def _layernorm(x, axis=-1, eps=1e-5):
    m = x.mean(dim=axis, keepdim=True)
    v = x.var(dim=axis, keepdim=True, unbiased=False)
    return (x - m) / torch.sqrt(v + eps)
def _scatter_add(tgt, idx, src, axis=0):
    return tgt.clone().index_add_(axis, idx, src)
def _segment_sum(src, seg, n):
    out = torch.zeros((n,) + src.shape[1:], dtype=src.dtype)
    return out.index_add_(0, seg, src)
def _pad(x, pw):
    flat = tuple(v for pair in reversed(pw) for v in pair)
    return torch.nn.functional.pad(x, flat)

kernel_name = 'gated_mix__mha_block__sliding_local_dilated__prompt'

def setup_inputs():

    _w = (lambda a: (lambda t: t.to(torch.bfloat16) if t.is_floating_point() else t)(torch.as_tensor(a)))
    def _A_setup():
        B, S, D, H = 1, 2048, 512, 32
        return {
            "x": _w((lambda *s: torch.randn(*s, dtype=torch.bfloat16) * 0.05)(B, S, D)),
            "wqkv": _w((lambda *s: torch.randn(*s, dtype=torch.bfloat16) * 1.6)(D, 3 * D)),
            "wo": _w((lambda *s: torch.randn(*s, dtype=torch.bfloat16) * 0.1)(D, D)),
        }
    _A_in = _A_setup()

    def _B_setup():
        B, S, D = 1, 2048, 512
        return {
            "x": _w((lambda *s: torch.randn(*s, dtype=torch.bfloat16) * 0.05)(B, S, D)),
            "wqkv": _w((lambda *s: torch.randn(*s, dtype=torch.bfloat16) * 1.6)(D, 3 * D)),
            "wo": _w((lambda *s: torch.randn(*s, dtype=torch.bfloat16) * 0.05)(D, D)),
        }
    _B_in = _B_setup()

    return {
        "A_x": _A_in["x"],
        "A_wqkv": _A_in["wqkv"],
        "A_wo": _A_in["wo"],
        "B_x": _B_in["x"],
        "B_wqkv": _B_in["wqkv"],
        "B_wo": _B_in["wo"],
        "gate_w": _w((lambda *s: torch.randn(*s, dtype=torch.bfloat16) * 0.05)(512) * 16.0),
    }

def reference(A_x, A_wqkv, A_wo, B_x, B_wqkv, B_wo, gate_w):
    def _A_ref(x, wqkv, wo):
        B, S, D = x.shape
        H = 32
        Dh = D // H
        qkv = (lambda x, *d: x.permute(*d))((x @ wqkv).reshape(B, S, 3, H, Dh), 2, 0, 3, 1, 4)
        q, k, v = qkv[0], qkv[1], qkv[2]
        scores = torch.einsum("bhqd,bhkd->bhqk", q, k) * (Dh ** -0.5)
        p = _softmax(scores, axis=-1)
        o = torch.einsum("bhqk,bhkd->bhqd", p, v)
        return (lambda x, *d: x.permute(*d))(o, 0, 2, 1, 3).reshape(B, S, D) @ wo

    def _B_ref(x, wqkv, wo):
        B, S, D = x.shape
        H = 32; Dh = D // H
        W, dil = 8, 2
        qkv = (lambda x, *d: x.permute(*d))((x @ wqkv).reshape(B, S, 3, H, Dh), 2, 0, 3, 1, 4)
        q, k, v = qkv[0], qkv[1], qkv[2]
        scale = Dh ** -0.5
        pos = torch.arange(S)
        off = torch.arange(W) * dil
        idx = pos[:, None] - off[None, :]
        mask = idx >= 0
        idx_c = torch.where(mask, idx, 0).reshape(-1)
        kg = k[:, :, idx_c, :].reshape(B, H, S, W, Dh)
        vg = v[:, :, idx_c, :].reshape(B, H, S, W, Dh)
        logits = torch.einsum("bhsd,bhswd->bhsw", q, kg) * scale
        logits = torch.where(mask[None, None, :, :], logits, -1e9)
        attn = _softmax(logits, axis=-1)
        o = torch.einsum("bhsw,bhswd->bhsd", attn, vg)
        return (lambda x, *d: x.permute(*d))(o, 0, 2, 1, 3).reshape(B, S, D) @ wo

    x = A_x
    g = _sigmoid((x * gate_w).sum(-1, keepdims=True))
    a = _A_ref(x, A_wqkv, A_wo)
    b = _B_ref(x, B_wqkv, B_wo)
    return g * a + (1 - g) * b
```

```cpp
#include <hip/hip_runtime.h>
#include <hip/hip_bf16.h>

typedef unsigned short u16;
__device__ __forceinline__ float bf2f(u16 v) { return __uint_as_float(((unsigned)v) << 16); }
__device__ __forceinline__ u16 f2bf(float f) { unsigned u = __float_as_uint(f); u += 0x7fffu + ((u >> 16) & 1u); return (u16)(u >> 16); }

constexpr int S = 2048, D = 512, H = 32, DH = 16;

__global__ void gemm_naive(const u16* A, const u16* B, u16* C, int M, int N, int K) {
  int n = blockIdx.x * blockDim.x + threadIdx.x;
  int m = blockIdx.y;
  float acc = 0.f;
  for (int k = 0; k < K; ++k) acc += bf2f(A[m * K + k]) * bf2f(B[k * N + n]);
  C[m * N + n] = f2bf(acc);
}

__global__ void attn_naive(const u16* qkv, u16* o) {
  int q = blockIdx.x, h = blockIdx.y, t = threadIdx.x;
  __shared__ float sc[S];
  __shared__ float red[64];
  float qv[DH];
  for (int d = 0; d < DH; ++d) qv[d] = bf2f(qkv[q * 1536 + h * 16 + d]);
  float mx = -1e30f;
  for (int j = t; j < S; j += 64) {
    float s = 0;
    for (int d = 0; d < DH; ++d) s += qv[d] * bf2f(qkv[j * 1536 + 512 + h * 16 + d]);
    s *= 0.25f; sc[j] = s; mx = fmaxf(mx, s);
  }
  red[t] = mx; __syncthreads();
  for (int i = 0; i < 64; ++i) mx = fmaxf(mx, red[i]);
  __syncthreads();
  float l = 0;
  for (int j = t; j < S; j += 64) { float p = __expf(sc[j] - mx); sc[j] = p; l += p; }
  red[t] = l; __syncthreads();
  l = 0; for (int i = 0; i < 64; ++i) l += red[i];
  __syncthreads();
  if (t < DH) {
    float acc = 0;
    for (int j = 0; j < S; ++j) acc += sc[j] * bf2f(qkv[j * 1536 + 1024 + h * 16 + t]);
    o[q * D + h * 16 + t] = f2bf(acc / l);
  }
}

__global__ void attn_local(const u16* qkv, u16* o) {
  int idx = blockIdx.x * blockDim.x + threadIdx.x;
  int s = idx / H, h = idx % H;
  float lg[8]; float mx = -1e30f;
  for (int w = 0; w < 8; ++w) {
    int j = s - 2 * w;
    if (j >= 0) {
      float a = 0;
      for (int d = 0; d < DH; ++d) a += bf2f(qkv[s * 1536 + h * 16 + d]) * bf2f(qkv[j * 1536 + 512 + h * 16 + d]);
      lg[w] = a * 0.25f;
    } else lg[w] = -1e9f;
    mx = fmaxf(mx, lg[w]);
  }
  float l = 0;
  for (int w = 0; w < 8; ++w) { lg[w] = __expf(lg[w] - mx); l += lg[w]; }
  for (int d = 0; d < DH; ++d) {
    float a = 0;
    for (int w = 0; w < 8; ++w) { int j = s - 2 * w; if (j < 0) j = 0; a += lg[w] * bf2f(qkv[j * 1536 + 1024 + h * 16 + d]); }
    o[s * D + h * 16 + d] = f2bf(a / l);
  }
}

__global__ void final_mix(const u16* x, const u16* gw, const u16* oa, const u16* ob, const u16* woa, const u16* wob, u16* out) {
  int n = blockIdx.x * blockDim.x + threadIdx.x;
  int m = blockIdx.y;
  float g = 0;
  for (int k = 0; k < D; ++k) g += bf2f(x[m * D + k]) * bf2f(gw[k]);
  g = 1.f / (1.f + __expf(-g));
  float a = 0, b = 0;
  for (int k = 0; k < D; ++k) { a += bf2f(oa[m * D + k]) * bf2f(woa[k * D + n]); b += bf2f(ob[m * D + k]) * bf2f(wob[k * D + n]); }
  out[m * D + n] = f2bf(g * a + (1.f - g) * b);
}

extern "C" void kernel_launch(void* const* d_in, const int* in_sizes, int n_in,
                              void* d_out, int out_size, void* d_ws, size_t ws_size,
                              hipStream_t stream) {
  const u16* x = (const u16*)d_in[0];
  const u16* wqkvA = (const u16*)d_in[1];
  const u16* woA = (const u16*)d_in[2];
  const u16* wqkvB = (const u16*)d_in[4];
  const u16* woB = (const u16*)d_in[5];
  const u16* gw = (const u16*)d_in[6];
  u16* ws = (u16*)d_ws;
  u16* qkvA = ws;
  u16* qkvB = qkvA + S * 1536;
  u16* oA = qkvB + S * 1536;
  u16* oB = oA + S * D;
  gemm_naive<<<dim3(1536 / 256, S), 256, 0, stream>>>(x, wqkvA, qkvA, S, 1536, D);
  gemm_naive<<<dim3(1536 / 256, S), 256, 0, stream>>>(x, wqkvB, qkvB, S, 1536, D);
  attn_naive<<<dim3(S, H), 64, 0, stream>>>(qkvA, oA);
  attn_local<<<S * H / 256, 256, 0, stream>>>(qkvB, oB);
  final_mix<<<dim3(D / 256, S), 256, 0, stream>>>(x, gw, oA, oB, woA, woB, (u16*)d_out);
}
```

```cpp
#include <hip/hip_runtime.h>
#include <hip/hip_bf16.h>
#include <stdint.h>

#define FUSE 1

typedef unsigned long long u64;
using f32x16 = __attribute__((ext_vector_type(16))) float;
using bf16x8 = __attribute__((ext_vector_type(8))) short;
using s16x4 = __attribute__((ext_vector_type(4))) short;
using u32x4 = __attribute__((ext_vector_type(4))) unsigned;
typedef __bf16 bf2_t __attribute__((ext_vector_type(2)));
typedef float f2_t __attribute__((ext_vector_type(2)));
#define GAS __attribute__((address_space(1)))
#define LAS __attribute__((address_space(3)))
#define CAS __attribute__((address_space(4)))
#define MFMA(a, b, c) __builtin_amdgcn_mfma_f32_32x32x16_bf16((a), (b), (c), 0, 0, 0)
#define RLX_AGENT __ATOMIC_RELAXED, __HIP_MEMORY_SCOPE_AGENT
#define BAR() asm volatile("s_barrier" ::: "memory")
#define WAITV(n) asm volatile("s_waitcnt vmcnt(" #n ")" ::: "memory")
#define WAITL0() asm volatile("s_waitcnt lgkmcnt(0)" ::: "memory")
#define SETP(x) __builtin_amdgcn_s_setprio(x)

constexpr size_t WS_NONCE = 0;
constexpr size_t WS_FA1 = 4096, WS_FB1 = 8192, WS_FB2 = 12288, WS_FA2 = 16384, WS_TMO = 20480;
constexpr size_t WS_G = 32768;
constexpr size_t MB = 1u << 20;
constexpr size_t WS_QA = 1 * MB, WS_KA = 3 * MB, WS_VA = 5 * MB, WS_QB = 7 * MB, WS_KB = 9 * MB, WS_VB = 11 * MB, WS_OA = 13 * MB, WS_OB = 15 * MB;
constexpr unsigned FRAG_BYTES = 2u << 20;
constexpr int LDS_TOTAL = 160 * 1024;
constexpr int L_X = 0, L_RING = 131072;
constexpr int L_K = 0, L_V = 65536, L_ONE = 131072, L_RED = L_ONE + 4096, L_MISC = L_RED + 8 * 9 * 256;
constexpr int L_WB = 0, L_WA = 65536, L_RED3 = 131072;
static_assert(L_MISC + 256 <= LDS_TOTAL, "lds");
constexpr float QSCALE = 0.25f * 1.4426950408889634f;
constexpr int NT0 = 30;
constexpr unsigned SPIN_CAP = 1u << 20;

struct Params {
  const char* x; const char* wA; const char* woA; const char* wB; const char* woB; const char* gw; char* out; char* ws;
};

__device__ __forceinline__ unsigned cvtpk(float lo, float hi) {
  f2_t v = {lo, hi};
  return __builtin_bit_cast(unsigned, __builtin_convertvector(v, bf2_t));
}
__device__ __forceinline__ u32x4 pack8(const f32x16& a, int base, float s) {
  u32x4 r;
#pragma unroll
  for (int i = 0; i < 4; ++i) r[i] = cvtpk(a[base + 2 * i] * s, a[base + 2 * i + 1] * s);
  return r;
}
__device__ __forceinline__ u32x4 pack8n(const f32x16& a, int base) {
  u32x4 r;
#pragma unroll
  for (int i = 0; i < 4; ++i) r[i] = cvtpk(a[base + 2 * i], a[base + 2 * i + 1]);
  return r;
}
template <bool F> __device__ __forceinline__ void st16(__amdgpu_buffer_rsrc_t r, unsigned off, u32x4 v) {
  __builtin_amdgcn_raw_buffer_store_b128(v, r, off, 0, F ? 16 : 0);
}
template <bool F> __device__ __forceinline__ u32x4 ld16(__amdgpu_buffer_rsrc_t r, unsigned off) {
  return __builtin_amdgcn_raw_buffer_load_b128(r, off, 0, F ? 16 : 0);
}
__device__ __forceinline__ __amdgpu_buffer_rsrc_t mkrs(const void* p, unsigned bytes) {
  return __builtin_amdgcn_make_buffer_rsrc((void*)p, 0, bytes, 0x00020000);
}
__device__ __forceinline__ bf16x8 lds_b128(char* lds, unsigned a) { return *(LAS const bf16x8*)(lds + a); }
__device__ __forceinline__ bf16x8 lds_tr2(char* lds, unsigned a, unsigned second) {
  s16x4 lo = __builtin_amdgcn_ds_read_tr16_b64_v4i16((LAS s16x4*)(lds + a));
  s16x4 hi = __builtin_amdgcn_ds_read_tr16_b64_v4i16((LAS s16x4*)(lds + a + second));
  return __builtin_shufflevector(lo, hi, 0, 1, 2, 3, 4, 5, 6, 7);
}
__device__ __forceinline__ void glds16(const void* g, unsigned dst) {
  unsigned sv;
  asm volatile("s_mov_b32 %0, m0\n\ts_mov_b32 m0, %2\n\ts_nop 0\n\tglobal_load_lds_dwordx4 %1, off\n\ts_mov_b32 m0, %0" : "=&s"(sv) : "v"(g), "s"(dst) : "memory");
}
#define SB() __builtin_amdgcn_sched_barrier(0)
#define OPAQUE(x) asm volatile("" : "+v"(x))
#define STAMP(i)
#define CSTAMP(i)
__device__ __forceinline__ void poll_flag_at(char* ws, size_t off, int lane, u64 tag) {
  GAS u64* f = (GAS u64*)(ws + off);
  unsigned spins = 0;
  while (true) {
    u64 v = __hip_atomic_load(f, RLX_AGENT);
    if (__all(v == tag)) break;
    __builtin_amdgcn_s_sleep(1);
    if (++spins > SPIN_CAP) { if (lane == 0) __hip_atomic_store((GAS unsigned*)(ws + WS_TMO), 1u, RLX_AGENT); break; }
  }
}
__device__ __forceinline__ void poll_flags(char* ws, size_t off, int n, int lane, u64 tag) { poll_flag_at(ws, off + (lane < n ? lane : 0) * 8, lane, tag); }
__device__ __forceinline__ void set_flag(char* ws, size_t off, u64 tag) { __hip_atomic_store((GAS u64*)(ws + off), tag, RLX_AGENT); }

template <bool F>
__device__ __forceinline__ void p1_epilogue(const Params& p, const f32x16& aq, const f32x16& ak, const f32x16& av, int br, int T, int hp, int lane) {
  char* base = p.ws + (br ? WS_QB : WS_QA);
  auto rq = mkrs(base, FRAG_BYTES), rk = mkrs(base + 2 * MB, FRAG_BYTES), rv = mkrs(base + 4 * MB, FRAG_BYTES);
  const unsigned o0 = ((2 * hp) * 64 + T) * 1024 + lane * 16;
  st16<F>(rk, o0, pack8n(ak, 0));
  st16<F>(rk, o0 + 65536, pack8n(ak, 8));
  const unsigned ov = ((2 * hp + ((lane >> 4) & 1)) * 64 + T) * 1024 + (lane >> 5) * 256 + (lane & 15) * 16;
  st16<F>(rv, ov, pack8n(av, 0));
  st16<F>(rv, ov + 512, pack8n(av, 8));
  st16<F>(rq, o0, pack8(aq, 0, QSCALE));
  st16<F>(rq, o0 + 65536, pack8(aq, 8, QSCALE));
}

template <bool F>
__device__ __forceinline__ void phase1(const Params& p, char* lds, int wid, int lane, int tb, int hp, u64 tag) {
  const unsigned ldsb = (unsigned)(uintptr_t)(LAS char*)lds;
  if (wid < 8) {
    {
      const int row0 = 4 * wid + (lane >> 4);
      const char* src = p.x + (size_t)(tb * 128 + row0) * 1024 + (((lane & 15) ^ (row0 & 15)) * 16);
#pragma unroll
      for (int n = 0; n < 16; ++n)
        glds16(src + (n & 3) * 32768 + (n >> 2) * 256, ldsb + L_X + (n >> 2) * 32768 + (wid + 8 * (n & 3)) * 1024);
    }
    const int tt = wid & 3, br = wid >> 2;
    WAITV(12);
    BAR();
    if (br) {
#pragma unroll
      for (int s = 0; s < 16; ++s) {
        if (s == 2) WAITV(8);
        if (s == 6) WAITV(4);
        if (s == 10) WAITV(0);
        BAR();
      }
    }
    const int r32 = lane & 31, hh = lane >> 5, xr = r32 & 15;
    unsigned xa0 = L_X + (32 * tt + r32) * 256 + (((hh ^ (xr & 1)) | (xr & 14)) * 16);
    unsigned wa0 = L_RING + (8 * hh + ((lane & 15) >> 2)) * 64 + ((lane >> 4) & 1) * 32 + (lane & 3) * 8;
    OPAQUE(wa0);
    f32x16 aq = {}, ak = {}, av = {};
    bf16x8 xf[2], wq[2], wk[2], wv[2];
    auto ldfr = [&](int set, int ks) {
      const int slot = (ks >> 1) & 3, s2 = ks & 1;
      xf[set] = lds_b128(lds, (xa0 ^ ((ks & 7) << 5)) + (ks >> 3) * 32768);
      const unsigned w = wa0 + slot * 6144 + s2 * 1024;
      wq[set] = lds_tr2(lds, w, 256);
      wk[set] = lds_tr2(lds, w + 2048, 256);
      wv[set] = lds_tr2(lds, w + 4096, 256);
    };
    STAMP(1);
    ldfr(0, 0);
#pragma unroll
    for (int s = 0; s < 16; ++s) {
      ldfr(1, 2 * s + 1);
      SB();
      aq = MFMA(wq[0], xf[0], aq); ak = MFMA(wk[0], xf[0], ak); av = MFMA(xf[0], wv[0], av);
      SB();
      if (s < 15) ldfr(0, 2 * s + 2);
      SB();
      aq = MFMA(wq[1], xf[1], aq); ak = MFMA(wk[1], xf[1], ak); av = MFMA(xf[1], wv[1], av);
      SB();
      if (s == 2) WAITV(8);
      if (s == 6) WAITV(4);
      if (s == 10) WAITV(0);
      CSTAMP(2 * s);
      BAR();
      CSTAMP(2 * s + 1);
    }
    STAMP(2);
    p1_epilogue<F>(p, aq, ak, av, br, tb * 4 + tt, hp, lane);
    STAMP(3);
    if (!br) {
#pragma unroll
      for (int s = 16; s < 32; ++s) {
        if (F && s == 27) WAITV(0);
        if (F && s == 28 && wid == 0 && lane == 0) set_flag(p.ws, WS_FA1 + (hp * 16 + tb) * 8, tag);
        BAR();
      }
    }
  } else if (wid < 14) {
    const int w = wid - 8;
    const unsigned goff = ((w & 1) * 16 + (lane >> 2)) * 3072 + ((w >> 1) * 512 + 32 * hp) * 2 + (lane & 3) * 16;
    auto rA = mkrs(p.wA, 512 * 3072), rB = mkrs(p.wB, 512 * 3072);
    u32x4 wr[16];
#pragma unroll
    for (int c = 0; c < 16; ++c) wr[c] = __builtin_amdgcn_raw_buffer_load_b128(rA, goff + c * 32 * 3072, 0, 0);
    LAS u32x4* ring = (LAS u32x4*)(lds + L_RING + w * 1024 + lane * 16);
#pragma unroll
    for (int c = 0; c < 2; ++c) {
      ring[c * 384] = wr[c];
      wr[c] = __builtin_amdgcn_raw_buffer_load_b128(rB, goff + c * 32 * 3072, 0, 0);
    }
    WAITL0();
    BAR();
#pragma unroll
    for (int s = 0; s < 32; ++s) {
      const int c = s + 2;
      if (c < 32) {
        ring[(c & 3) * 384] = wr[c & 15];
        if (c < 16) wr[c] = __builtin_amdgcn_raw_buffer_load_b128(rB, goff + c * 32 * 3072, 0, 0);
        WAITL0();
      }
      BAR();
    }
  } else {
    u32x4 gwv;
    if (wid == 14) gwv = *(const GAS u32x4*)(p.gw + lane * 16);
    BAR();
#pragma unroll 1
    for (int s = 0; s < 32; ++s) {
      if (s == 12 && wid == 14) {
        float mine = 0.f;
#pragma unroll
        for (int i = 0; i < 8; ++i) {
          const int row = 8 * hp + i;
          u32x4 xv = *(LAS const u32x4*)(lds + L_X + (lane >> 4) * 32768 + row * 256 + (((lane & 15) ^ (row & 15)) * 16));
          float a = 0.f;
#pragma unroll
          for (int q = 0; q < 4; ++q) {
            const unsigned xa = xv[q], ga = gwv[q];
            a = __builtin_amdgcn_fdot2_f32_bf16(__builtin_bit_cast(bf2_t, xa), __builtin_bit_cast(bf2_t, ga), a, false);
          }
#pragma unroll
          for (int d = 1; d < 64; d <<= 1) a += __shfl_xor(a, d);
          if (lane == i) mine = a;
        }
        const float g = 1.f / (1.f + __expf(-mine));
        if (lane < 8) __hip_atomic_store((GAS float*)(p.ws + WS_G) + tb * 128 + 8 * hp + lane, g, RLX_AGENT);
      }
      if (F && s == 27 && wid == 14) WAITV(0);
      BAR();
    }
  }
}

__device__ __forceinline__ void attn_tile(char* lds, unsigned ka, unsigned va, int off, const bf16x8& qf, f32x16& o, const f32x16& c) {
  bf16x8 kf = lds_b128(lds, ka + off);
  bf16x8 v0 = lds_b128(lds, va + off);
  bf16x8 v1 = lds_b128(lds, va + off + 512);
  f32x16 s = MFMA(kf, qf, c);
  u32x4 p0, p1;
#pragma unroll
  for (int i = 0; i < 4; ++i) p0[i] = cvtpk(__builtin_amdgcn_exp2f(s[2 * i]), __builtin_amdgcn_exp2f(s[2 * i + 1]));
#pragma unroll
  for (int i = 0; i < 4; ++i) p1[i] = cvtpk(__builtin_amdgcn_exp2f(s[8 + 2 * i]), __builtin_amdgcn_exp2f(s[8 + 2 * i + 1]));
  o = MFMA(v0, __builtin_bit_cast(bf16x8, p0), o);
  o = MFMA(v1, __builtin_bit_cast(bf16x8, p1), o);
}

template <bool F>
__device__ __forceinline__ void local_unit(const Params& p, int h, int T, int lane, float g) {
  auto rq = mkrs(p.ws + WS_QB, FRAG_BYTES), rk = mkrs(p.ws + WS_KB, FRAG_BYTES), rv = mkrs(p.ws + WS_VB, FRAG_BYTES);
  const int Tp = T > 0 ? T - 1 : 0;
  const int r32 = lane & 31, hh = lane >> 5;
  const unsigned oc = (h * 64 + T) * 1024, op = (h * 64 + Tp) * 1024;
  u32x4 q = ld16<F>(rq, oc + lane * 16), kc = ld16<F>(rk, oc + lane * 16), kp = ld16<F>(rk, op + lane * 16);
  const unsigned vo = hh * 256 + (r32 & 15) * 16;
  u32x4 v0 = ld16<F>(rv, oc + vo), v1 = ld16<F>(rv, oc + vo + 512), vp = ld16<F>(rv, op + vo + 512);
  if (r32 >= 16) {
    const unsigned o = (r32 == 16 || r32 == 20) ? 0x3F803F80u : 0u;
    v0 = u32x4{o, o, o, o}; v1 = v0; vp = v0;
  }
  u64 m = 0x5555ull << (18 + r32);
  if (T == 0) m &= 0xFFFFFFFF00000000ull;
  m >>= 4 * hh;
  const unsigned mlo = (unsigned)m, mhi = (unsigned)(m >> 32);
  const float NEG = -1e30f;
  f32x16 cc, cp;
#pragma unroll
  for (int i = 0; i < 16; ++i) {
    const int b = (i & 3) + 8 * (i >> 2);
    cc[i] = ((mhi >> b) & 1u) ? 0.f : NEG;
    cp[i] = (i >= 8 && ((mlo >> b) & 1u)) ? 0.f : NEG;
  }
  f32x16 sc = MFMA(__builtin_bit_cast(bf16x8, kc), __builtin_bit_cast(bf16x8, q), cc);
  f32x16 sp = MFMA(__builtin_bit_cast(bf16x8, kp), __builtin_bit_cast(bf16x8, q), cp);
  float mx = sc[0];
#pragma unroll
  for (int i = 1; i < 16; ++i) mx = fmaxf(mx, sc[i]);
#pragma unroll
  for (int i = 8; i < 16; ++i) mx = fmaxf(mx, sp[i]);
  {
    auto rr = __builtin_amdgcn_permlane32_swap(__float_as_uint(mx), __float_as_uint(mx), false, false);
    mx = fmaxf(__uint_as_float(rr[0]), __uint_as_float(rr[1]));
  }
  u32x4 p0, p1, pp;
#pragma unroll
  for (int i = 0; i < 4; ++i) {
    p0[i] = cvtpk(__builtin_amdgcn_exp2f(sc[2 * i] - mx), __builtin_amdgcn_exp2f(sc[2 * i + 1] - mx));
    p1[i] = cvtpk(__builtin_amdgcn_exp2f(sc[8 + 2 * i] - mx), __builtin_amdgcn_exp2f(sc[8 + 2 * i + 1] - mx));
    pp[i] = cvtpk(__builtin_amdgcn_exp2f(sp[8 + 2 * i] - mx), __builtin_amdgcn_exp2f(sp[8 + 2 * i + 1] - mx));
  }
  f32x16 o = {};
  o = MFMA(__builtin_bit_cast(bf16x8, v0), __builtin_bit_cast(bf16x8, p0), o);
  o = MFMA(__builtin_bit_cast(bf16x8, v1), __builtin_bit_cast(bf16x8, p1), o);
  o = MFMA(__builtin_bit_cast(bf16x8, vp), __builtin_bit_cast(bf16x8, pp), o);
  const float sc2 = (1.f - g) * __builtin_amdgcn_rcpf(o[8]);
  st16<F>(mkrs(p.ws + WS_OB, FRAG_BYTES), (T * 32 + h) * 1024 + lane * 16, pack8(o, 0, sc2));
}

template <bool F>
__device__ __forceinline__ void phase2(const Params& p, char* lds, int wid, int lane, int tid, int h, int j, u64 tag) {
  if (F) {
    if (wid == 15) poll_flags(p.ws, WS_FA1 + (h >> 1) * 128, 16, lane, tag);
    BAR();
  }
  const int qt = wid & 7, kh = wid >> 3, r32 = lane & 31, hh = lane >> 5;
  auto rq = mkrs(p.ws + WS_QA + (size_t)h * 65536, 65536), rk = mkrs(p.ws + WS_KA + (size_t)h * 65536, 65536), rv = mkrs(p.ws + WS_VA + (size_t)h * 65536, 65536);
  const bf16x8 qf = __builtin_bit_cast(bf16x8, ld16<F>(rq, (j * 8 + qt) * 1024 + lane * 16));
  u32x4 pc[8]; unsigned pd[8];
  {
    const int t0 = (wid & 3) + ((wid >> 2) & 1) * NT0;
    pc[0] = ld16<F>(kh ? rv : rk, t0 * 1024 + lane * 16);
    pd[0] = (kh ? L_V : L_K) + t0 * 1024 + lane * 16;
#pragma unroll
    for (int n = 1; n < 8; ++n) {
      const int i = (n - 1) * 16 + wid, ti = i >> 1;
      const int t = ti < NT0 - 4 ? 4 + ti : 8 + ti;
      pc[n] = ld16<F>((wid & 1) ? rv : rk, t * 1024 + lane * 16);
      pd[n] = ((wid & 1) ? L_V : L_K) + t * 1024 + lane * 16;
    }
  }
  {
    const unsigned v = ((tid >> 2) & 15) == 0 || ((tid >> 2) & 15) == 4 ? 0x3F803F80u : 0u;
    *(LAS unsigned*)(lds + L_ONE + tid * 4) = v;
    if (tid < 2) *(LAS unsigned*)(lds + L_MISC + tid * 4) = 0u;
  }
  unsigned kaddr = L_K + kh * NT0 * 1024 + lane * 16, vaddr, vstep;
  if (r32 < 16) { vaddr = L_V + kh * NT0 * 1024 + hh * 256 + r32 * 16; vstep = 2048; }
  else { vaddr = L_ONE + hh * 256 + ((r32 >= 20 && r32 < 28) ? 64 : 0) + ((r32 == 16 || r32 == 20) ? 0 : 16); vstep = 0; }
  const unsigned kaddr0 = kaddr, vaddr0 = vaddr;
  f32x16 o = {};
  const f32x16 zero = {};
  STAMP(9);
  *(LAS u32x4*)(lds + pd[0]) = pc[0];
  WAITL0();
  BAR();
  STAMP(10);
  SETP(3); attn_tile(lds, kaddr, vaddr, 0, qf, o, zero);
  *(LAS u32x4*)(lds + pd[1]) = pc[1]; *(LAS u32x4*)(lds + pd[2]) = pc[2];
  SETP(2); attn_tile(lds, kaddr, vaddr, 1024, qf, o, zero);
  *(LAS u32x4*)(lds + pd[3]) = pc[3]; *(LAS u32x4*)(lds + pd[4]) = pc[4];
  kaddr += 2048; vaddr += vstep;
  SETP(1); attn_tile(lds, kaddr, vaddr, 0, qf, o, zero);
  *(LAS u32x4*)(lds + pd[5]) = pc[5]; *(LAS u32x4*)(lds + pd[6]) = pc[6];
  SETP(0); attn_tile(lds, kaddr, vaddr, 1024, qf, o, zero);
  *(LAS u32x4*)(lds + pd[7]) = pc[7];
  kaddr += 2048; vaddr += vstep;
  if (F) {
    if (wid >= 4 && wid < 8) WAITV(0);
  }
  WAITL0();
  STAMP(11);
  BAR();
  if (F) {
    if (wid == 4 && lane == 0) {
      const int b = blockIdx.x, xcd = b & 7, idx = b >> 3;
      set_flag(p.ws, WS_FB1 + (((xcd >> 1) * 4 + (idx >> 3)) * 16 + (xcd & 1) * 8 + (idx & 7)) * 8, tag);
    }
  }
  SETP(3);
  float g = 0.f;
  if (kh == 0) {
    if (F) {
      const int fi = lane < 32 ? (lane & 15) * 16 + 2 * j + (lane >> 4) : (h >> 1) * 16 + (j > 0 ? 2 * j - 1 : 0);
      poll_flag_at(p.ws, WS_FB1 + fi * 8, lane, tag);
    }
    g = __hip_atomic_load((GAS float*)(p.ws + WS_G) + j * 256 + qt * 32 + r32, RLX_AGENT);
    local_unit<F>(p, h, j * 8 + qt, lane, g);
    if (F) {
      WAITV(0);
      unsigned old = 0;
      if (lane == 0) old = __hip_atomic_fetch_add((LAS unsigned*)(lds + L_MISC), 1u, __ATOMIC_RELAXED, __HIP_MEMORY_SCOPE_WORKGROUP);
      if (lane == 0 && old == 7) set_flag(p.ws, WS_FB2 + (j * 32 + h) * 8, tag);
    }
  }
  STAMP(12);
  const int nit = kh ? (60 - NT0) / 2 : (NT0 - 4) / 2;
#pragma unroll 1
  for (int it = nit; it > 0; --it) {
    if (it == 6) SETP(2);
    if (it == 3) SETP(1);
    if (it == 1) SETP(0);
    attn_tile(lds, kaddr, vaddr, 0, qf, o, zero);
    attn_tile(lds, kaddr, vaddr, 1024, qf, o, zero);
    kaddr += 2048; vaddr += vstep;
  }
  STAMP(13);
  LAS float* red = (LAS float*)(lds + L_RED);
  auto rO = mkrs(p.ws + WS_OA, FRAG_BYTES);
  const unsigned oo = ((j * 8 + qt) * 32 + h) * 1024 + lane * 16;
  if (kh == 1) {
#pragma unroll
    for (int i = 0; i < 9; ++i) red[(qt * 9 + i) * 64 + lane] = o[i];
  }
  WAITL0();
  BAR();
  if (kh == 0) {
    bool ok = true;
#pragma unroll
    for (int i = 0; i < 9; ++i) { o[i] += red[(qt * 9 + i) * 64 + lane]; ok = ok && (fabsf(o[i]) <= 3.0e38f); }
    ok = ok && (o[8] >= 8.67e-19f);
    if (!__all(ok)) { if (lane == 0) *(LAS unsigned*)(lds + L_MISC + 4) = 1u; }
    else st16<F>(rO, oo, pack8(o, 0, g * __builtin_amdgcn_rcpf(o[8])));
    WAITV(0);
  }
  WAITL0();
  BAR();
  STAMP(14);
  if (*(LAS volatile unsigned*)(lds + L_MISC + 4)) {
    const int nt = kh ? 64 - NT0 : NT0;
    float mx = -3.0e38f;
    for (int t = 0; t < nt; ++t) {
      f32x16 s = MFMA(lds_b128(lds, kaddr0 + t * 1024), qf, zero);
#pragma unroll
      for (int i = 0; i < 16; ++i) mx = fmaxf(mx, s[i]);
    }
    { auto rr = __builtin_amdgcn_permlane32_swap(__float_as_uint(mx), __float_as_uint(mx), false, false);
      mx = fmaxf(__uint_as_float(rr[0]), __uint_as_float(rr[1])); }
    red[wid * 64 + lane] = mx;
    WAITL0(); BAR();
    mx = fmaxf(mx, red[(wid ^ 8) * 64 + lane]);
    WAITL0(); BAR();
    f32x16 c;
#pragma unroll
    for (int i = 0; i < 16; ++i) { c[i] = -mx; o[i] = 0.f; }
    unsigned va = vaddr0;
    for (int t = 0; t < nt; ++t) { attn_tile(lds, kaddr0 + t * 1024, va, 0, qf, o, c); va += vstep >> 1; }
    if (kh == 1) {
#pragma unroll
      for (int i = 0; i < 9; ++i) red[(qt * 9 + i) * 64 + lane] = o[i];
    }
    WAITL0(); BAR();
    if (kh == 0) {
#pragma unroll
      for (int i = 0; i < 9; ++i) o[i] += red[(qt * 9 + i) * 64 + lane];
      st16<F>(rO, oo, pack8(o, 0, g * __builtin_amdgcn_rcpf(o[8])));
      WAITV(0);
    }
    WAITL0(); BAR();
  }
  if (F && tid == 0) set_flag(p.ws, WS_FA2 + (j * 32 + h) * 8, tag);
}

template <bool F>
__device__ __forceinline__ void phase3(const Params& p, char* lds, int wid, int lane, int tb, int cb, u64 tag) {
  const unsigned ldsb = (unsigned)(uintptr_t)(LAS char*)lds;
  if (wid >= 8) {
    const int w = wid - 8;
    const int row = lane >> 3, pos = (lane & 7) ^ (((row >> 1) & 1) << 2);
#pragma unroll
    for (int n = 0; n < 16; ++n) {
      const int br = n < 8 ? 1 : 0, blk = (n & 7) * 8 + w;
      const char* src = (br ? p.woB : p.woA) + (size_t)(blk * 8 + row) * 1024 + cb * 128 + pos * 16;
      glds16(src, ldsb + (br ? L_WB : L_WA) + blk * 1024);
    }
    WAITV(8);
    BAR();
    WAITV(0);
    BAR();
    BAR();
  } else {
    const int tt = wid & 1, ct = (wid >> 1) & 1, kq = wid >> 2;
    const int T = tb * 2 + tt, j = tb >> 2;
    const int hh = lane >> 5, q = (lane & 15) >> 2;
    const unsigned wa = (4 * hh + q) * 128 + ((ct ^ ((q >> 1) & 1)) * 64) + ((lane >> 4) & 1) * 32 + (lane & 3) * 8 + kq * 16 * 2048;
    f32x16 acc = {};
    u32x4 of[16];
    if (F) poll_flags(p.ws, WS_FB2 + (j * 32 + kq * 16) * 8, 16, lane, tag);
    {
      auto r = mkrs(p.ws + WS_OB, FRAG_BYTES);
#pragma unroll
      for (int i = 0; i < 16; ++i) of[i] = ld16<F>(r, (T * 32 + kq * 16 + i) * 1024 + lane * 16);
    }
    STAMP(17);
    BAR();
    STAMP(18);
#pragma unroll
    for (int i = 0; i < 16; ++i) acc = MFMA(lds_tr2(lds, L_WB + wa + i * 2048, 1024), __builtin_bit_cast(bf16x8, of[i]), acc);
    STAMP(19);
    if (F) poll_flags(p.ws, WS_FA2 + (j * 32 + kq * 16) * 8, 16, lane, tag);
    {
      auto r = mkrs(p.ws + WS_OA, FRAG_BYTES);
#pragma unroll
      for (int i = 0; i < 16; ++i) of[i] = ld16<F>(r, (T * 32 + kq * 16 + i) * 1024 + lane * 16);
    }
    STAMP(20);
    BAR();
#pragma unroll
    for (int i = 0; i < 16; ++i) acc = MFMA(lds_tr2(lds, L_WA + wa + i * 2048, 1024), __builtin_bit_cast(bf16x8, of[i]), acc);
    STAMP(21);
    LAS float* red = (LAS float*)(lds + L_RED3) + (wid & 3) * 1024;
    if (kq == 1) {
#pragma unroll
      for (int i = 0; i < 16; ++i) red[i * 64 + lane] = acc[i];
    }
    WAITL0();
    BAR();
    if (kq == 0) {
#pragma unroll
      for (int i = 0; i < 16; ++i) acc[i] += red[i * 64 + lane];
      unsigned w[8];
#pragma unroll
      for (int i = 0; i < 8; ++i) w[i] = cvtpk(acc[2 * i], acc[2 * i + 1]);
      char* dst = p.out + (size_t)(T * 32 + (lane & 31)) * 1024 + cb * 128 + ct * 64 + hh * 16;
#pragma unroll
      for (int g = 0; g < 4; g += 2) {
        auto a = __builtin_amdgcn_permlane32_swap(w[2 * g], w[2 * g + 2], false, false);
        auto b = __builtin_amdgcn_permlane32_swap(w[2 * g + 1], w[2 * g + 3], false, false);
        *(GAS u32x4*)(dst + g * 16) = u32x4{a[0], b[0], a[1], b[1]};
      }
    }
  }
}

template <int PH>
__global__ __launch_bounds__(1024) void gated_mix_kernel(Params p) {
  extern __shared__ __attribute__((aligned(16))) char lds[];
  const int tid = threadIdx.x, lane = tid & 63;
  const int wid = __builtin_amdgcn_readfirstlane(tid >> 6);
  const int b = blockIdx.x, xcd = b & 7, idx = b >> 3;
  u64 tag = 0;
  if (PH == 0) tag = *(const CAS u64*)(p.ws + WS_NONCE) + 1;
  STAMP(0 + (PH >= 2 ? (PH - 1) * 8 : 0));
  if (PH == 0 || PH == 1) phase1<PH == 0>(p, lds, wid, lane, (xcd & 1) * 8 + (idx & 7), (xcd >> 1) * 4 + (idx >> 3), tag);
  if (PH == 0 || PH == 1) STAMP(4);
  if (PH == 0 || PH == 2) phase2<PH == 0>(p, lds, wid, lane, tid, xcd * 4 + (idx >> 3), idx & 7, tag);
  if (PH == 0 || PH == 2) STAMP(15);
  if (PH == 0 || PH == 3) phase3<PH == 0>(p, lds, wid, lane, xcd * 4 + (idx >> 3), idx & 7, tag);
  if (PH == 0 || PH == 3) STAMP(23);
  if (PH == 0 && b == 0 && tid == 0) set_flag(p.ws, WS_NONCE, tag);
}

extern "C" void kernel_launch(void* const* d_in, const int* in_sizes, int n_in, void* d_out, int out_size, void* d_ws, size_t ws_size,
                              hipStream_t stream) {
  Params p;
  p.x = (const char*)d_in[0]; p.wA = (const char*)d_in[1]; p.woA = (const char*)d_in[2];
  p.wB = (const char*)d_in[4]; p.woB = (const char*)d_in[5]; p.gw = (const char*)d_in[6];
  p.out = (char*)d_out; p.ws = (char*)d_ws;
  (void)hipFuncSetAttribute((const void*)gated_mix_kernel<0>, hipFuncAttributeMaxDynamicSharedMemorySize, LDS_TOTAL);
  gated_mix_kernel<0><<<256, 1024, LDS_TOTAL, stream>>>(p);
}
```

```cpp
#include <hip/hip_runtime.h>
#include <hip/hip_bf16.h>
#include <stdint.h>
#include <type_traits>

#define FUSE 1

typedef unsigned long long u64;
using f32x16 = __attribute__((ext_vector_type(16))) float;
using bf16x8 = __attribute__((ext_vector_type(8))) short;
using s16x4 = __attribute__((ext_vector_type(4))) short;
using u32x4 = __attribute__((ext_vector_type(4))) unsigned;
typedef __bf16 bf2_t __attribute__((ext_vector_type(2)));
typedef float f2_t __attribute__((ext_vector_type(2)));
#define GAS __attribute__((address_space(1)))
#define LAS __attribute__((address_space(3)))
#define CAS __attribute__((address_space(4)))
#define MFMA(a, b, c) __builtin_amdgcn_mfma_f32_32x32x16_bf16((a), (b), (c), 0, 0, 0)
#define MFMA1(a, b, c) MFMA(a, b, c)
#define RLX_AGENT __ATOMIC_RELAXED, __HIP_MEMORY_SCOPE_AGENT
#define BAR() asm volatile("s_barrier" ::: "memory")
#define SBAR_STAGE() BAR()
#define WAITV(n) asm volatile("s_waitcnt vmcnt(" #n ")" ::: "memory")
#define WAITL0() asm volatile("s_waitcnt lgkmcnt(0)" ::: "memory")
#define SETP(x) __builtin_amdgcn_s_setprio(x)

constexpr size_t WS_NONCE = 0;
constexpr size_t WS_FA1 = 4096, WS_FB2 = 12288, WS_FA2 = 16384, WS_TMO = 20480;
constexpr size_t WS_G = 32768;
constexpr size_t MB = 1u << 20;
constexpr size_t WS_QA = 1 * MB, WS_KA = 3 * MB, WS_VA = 5 * MB, WS_OA = 7 * MB, WS_OB = 9 * MB;
constexpr unsigned FRAG_BYTES = 2u << 20;
constexpr int LDS_TOTAL = 160 * 1024;
constexpr int L_XR = 0, XSLOT = 160 * 128, L_WR = 3 * XSLOT, WSLOT = 3 * 64 * 128, L_GW = L_WR + 3 * WSLOT;
constexpr int L_BK = 0, L_BV = 20480, L_BQ = 40960;
static_assert(L_GW + 1024 <= 160 * 1024, "lds");
constexpr int L_K = 0, L_V = 65536, L_ONE = 131072, L_RED = L_ONE + 4096, L_MISC = L_RED + 8 * 9 * 256;
constexpr int L_WB = 0, L_WA = 65536, L_RED3 = 131072;
static_assert(L_MISC + 256 <= LDS_TOTAL, "lds");
constexpr float QSCALE = 0.25f * 1.4426950408889634f;
constexpr int NT0 = 32;
constexpr unsigned SPIN_CAP = 1u << 20;

struct Params {
  const char* x; const char* wA; const char* woA; const char* wB; const char* woB; const char* gw; char* out; char* ws;
};

__device__ __forceinline__ unsigned cvtpk(float lo, float hi) {
  f2_t v = {lo, hi};
  return __builtin_bit_cast(unsigned, __builtin_convertvector(v, bf2_t));
}
__device__ __forceinline__ u32x4 pack8(const f32x16& a, int base, float s) {
  u32x4 r;
#pragma unroll
  for (int i = 0; i < 4; ++i) r[i] = cvtpk(a[base + 2 * i] * s, a[base + 2 * i + 1] * s);
  return r;
}
__device__ __forceinline__ u32x4 pack8n(const f32x16& a, int base) {
  u32x4 r;
#pragma unroll
  for (int i = 0; i < 4; ++i) r[i] = cvtpk(a[base + 2 * i], a[base + 2 * i + 1]);
  return r;
}
template <bool F> __device__ __forceinline__ void st16(__amdgpu_buffer_rsrc_t r, unsigned off, u32x4 v) {
  __builtin_amdgcn_raw_buffer_store_b128(v, r, off, 0, F ? 16 : 0);
}
template <bool F> __device__ __forceinline__ u32x4 ld16(__amdgpu_buffer_rsrc_t r, unsigned off) {
  return __builtin_amdgcn_raw_buffer_load_b128(r, off, 0, F ? 16 : 0);
}
__device__ __forceinline__ __amdgpu_buffer_rsrc_t mkrs(const void* p, unsigned bytes) {
  return __builtin_amdgcn_make_buffer_rsrc((void*)p, 0, bytes, 0x00020000);
}
__device__ __forceinline__ bf16x8 lds_b128(char* lds, unsigned a) { return *(LAS const bf16x8*)(lds + a); }
__device__ __forceinline__ bf16x8 lds_tr2(char* lds, unsigned a, unsigned second) {
  s16x4 lo = __builtin_amdgcn_ds_read_tr16_b64_v4i16((LAS s16x4*)(lds + a));
  s16x4 hi = __builtin_amdgcn_ds_read_tr16_b64_v4i16((LAS s16x4*)(lds + a + second));
  return __builtin_shufflevector(lo, hi, 0, 1, 2, 3, 4, 5, 6, 7);
}
__device__ __forceinline__ void glds16(const void* g, unsigned dst) {
  unsigned sv;
  asm volatile("s_mov_b32 %0, m0\n\ts_mov_b32 m0, %2\n\ts_nop 0\n\tglobal_load_lds_dwordx4 %1, off\n\ts_mov_b32 m0, %0" : "=&s"(sv) : "v"(g), "s"(dst) : "memory");
}
__device__ __forceinline__ void glds16s(const void* sbase, unsigned voff, unsigned dst) {
  unsigned sv;
  asm volatile("s_mov_b32 %0, m0\n\ts_mov_b32 m0, %3\n\ts_nop 0\n\tglobal_load_lds_dwordx4 %1, %2\n\ts_mov_b32 m0, %0" : "=&s"(sv) : "v"(voff), "s"(sbase), "s"(dst) : "memory");
}
#define SB() __builtin_amdgcn_sched_barrier(0)
#define OPAQUE(x) asm volatile("" : "+v"(x))
#define STAMP(i)
#define CSTAMP(i)
__device__ __forceinline__ void poll_flag_at(char* ws, size_t off, int lane, u64 tag) {
  GAS u64* f = (GAS u64*)(ws + off);
  unsigned spins = 0;
  while (true) {
    u64 v = __hip_atomic_load(f, RLX_AGENT);
    if (__all(v == tag)) break;
    __builtin_amdgcn_s_sleep(1);
    if (++spins > SPIN_CAP) { if (lane == 0) __hip_atomic_store((GAS unsigned*)(ws + WS_TMO), 1u, RLX_AGENT); break; }
  }
}
__device__ __forceinline__ void poll_flags(char* ws, size_t off, int n, int lane, u64 tag) { poll_flag_at(ws, off + (lane < n ? lane : 0) * 8, lane, tag); }
__device__ __forceinline__ void set_flag(char* ws, size_t off, u64 tag) { __hip_atomic_store((GAS u64*)(ws + off), tag, RLX_AGENT); }

template <int N> __device__ __forceinline__ void waitv() { asm volatile("s_waitcnt vmcnt(%0)" :: "n"(N) : "memory"); }

struct P1Loader {
  const char* xs; const char* wsrc; unsigned xo, wo, ldsb; int lw, rot, halo_fix;
  __device__ __forceinline__ void init(const Params& p, unsigned ldsb_, int lw_, int lane, int br, int hq, int tb, int rot_) {
    ldsb = ldsb_; lw = lw_; rot = rot_;
    const int qs = (lane & 7) ^ ((lane >> 4) & 3) ^ (4 * (lw & 1));
    xs = p.x + (ptrdiff_t)(tb * 128 - 32) * 1024;
    xo = (lane >> 3) * 1024 + qs * 16;
    halo_fix = tb == 0 ? 32 * 1024 : 0;
    wsrc = (br ? p.wB : p.wA) + (size_t)(8 * lw) * 3072 + hq * 128;
    wo = (lane >> 3) * 3072 + (((lane & 7) ^ (((lane >> 4) & 1) << 2)) * 16);
  }
  template <bool HALO> __device__ __forceinline__ void unit(int u) {
    const int ku = (u + rot) & 7;
    const unsigned xd = ldsb + L_XR + (u % 3) * XSLOT, wd = ldsb + L_WR + (u % 3) * WSLOT + lw * 1024;
    glds16s(xs + (4 + lw) * 8192 + ku * 128, xo, xd + (4 + lw) * 1024);
    glds16s(wsrc + (size_t)ku * 64 * 3072, wo, wd);
    glds16s(xs + (12 + lw) * 8192 + ku * 128, xo, xd + (12 + lw) * 1024);
    glds16s(wsrc + (size_t)ku * 64 * 3072 + 1024, wo, wd + 8192);
    glds16s(wsrc + (size_t)ku * 64 * 3072 + 2048, wo, wd + 16384);
    if (HALO) glds16s(xs + lw * 8192 + ku * 128 + halo_fix, xo, xd + lw * 1024);
  }
};

__device__ __forceinline__ void local_unit(const Params& p, char* lds, int hl, int tt, int h, int T, int lane);

template <bool F>
__device__ __forceinline__ void phase1(const Params& p, char* lds, int wid, int lane, int br, int hq, int tb, int rot, int h2, u64 tagv) {
  const unsigned ldsb = (unsigned)(uintptr_t)(LAS char*)lds;
  const int r32 = lane & 31, hh = lane >> 5, q4 = (lane & 15) >> 2;
  const bool halo = br && wid >= 8 && wid < 12;
  {
    const bool main = wid < 8;
    const int tt = main ? (wid & 3) + 1 : 0, half = main ? wid >> 2 : (wid >> 1) & 1;
    const int row = 32 * tt + r32, f = (row >> 1) & 7;
    unsigned xa0 = L_XR + row * 128 + (((hh ^ (f & 1)) | (f & 6)) * 16);
    unsigned wa0 = L_WR + (8 * hh + q4) * 128 + ((half ^ ((q4 >> 1) & 1)) * 64) + ((lane >> 4) & 1) * 32 + (lane & 3) * 8;
    OPAQUE(wa0);
    auto ldx = [&](int ks) { return lds_b128(lds, (xa0 ^ ((ks & 3) << 5)) + ((ks >> 2) % 3) * XSLOT); };
    auto ldw = [&](int ks, int c) { return lds_tr2(lds, wa0 + ((ks >> 2) % 3) * WSLOT + (ks & 3) * 2048 + c * 8192, 512); };
    f32x16 aq = {}, ak = {}, av = {};
    if (main) {
      BAR();
      STAMP(1);
      bf16x8 xf[3], wq[3], wk[3], wv[3];
      xf[0] = ldx(0); wq[0] = ldw(0, 0); wk[0] = ldw(0, 1); wv[0] = ldw(0, 2);
      xf[1] = ldx(1); wq[1] = ldw(1, 0); wk[1] = ldw(1, 1); wv[1] = ldw(1, 2);
#pragma unroll
      for (int ks = 0; ks < 32; ++ks) {
        const int c = ks % 3, n = (ks + 2) % 3;
        SB();
        aq = MFMA1(wq[c], xf[c], aq);
        SB();
        if (ks < 30) { xf[n] = ldx(ks + 2); wq[n] = ldw(ks + 2, 0); }
        SB();
        ak = MFMA1(wk[c], xf[c], ak);
        SB();
        if (ks < 30) wk[n] = ldw(ks + 2, 1);
        SB();
        av = MFMA1(xf[c], wv[c], av);
        SB();
        if (ks < 30) wv[n] = ldw(ks + 2, 2);
        if (ks & 1) BAR();
      }
    } else if (halo) {
      SETP(3);
      P1Loader L; L.init(p, ldsb, wid - 8, lane, br, hq, tb, rot);
      L.unit<true>(0); L.unit<true>(1);
      waitv<6>();
      BAR();
      auto halo_loop = [&](auto ISV) {
        constexpr bool isv = decltype(ISV)::value;
        wa0 += isv ? 16384 : 8192;
        bf16x8 xf[3], wf[3];
        f32x16 a = {};
        xf[0] = ldx(0); wf[0] = ldw(0, 0); xf[1] = ldx(1); wf[1] = ldw(1, 0);
#pragma unroll
        for (int ks = 0; ks < 32; ++ks) {
          const int c = ks % 3, n = (ks + 2) % 3;
          if ((ks & 3) == 0 && ks / 4 + 2 < 8) L.unit<true>(ks / 4 + 2);
          SB();
          if (isv) a = MFMA1(xf[c], wf[c], a); else a = MFMA1(wf[c], xf[c], a);
          SB();
          if (ks < 30) { xf[n] = ldx(ks + 2); wf[n] = ldw(ks + 2, 0); }
          if ((ks & 3) == 1) { if (ks / 4 + 2 < 8) waitv<6>(); else waitv<0>(); }
          if (ks & 1) BAR();
        }
        return a;
      };
      if (wid & 1) av = halo_loop(std::true_type{}); else ak = halo_loop(std::false_type{});
    } else {
      SETP(3);
      P1Loader L; L.init(p, ldsb, wid - 8, lane, br, hq, tb, rot);
      const bool gatew = !br && wid == 8;
      if (gatew) glds16(p.gw + lane * 16, ldsb + L_GW);
      L.unit<false>(0); L.unit<false>(1);
      waitv<5>();
      BAR();
      float ga = 0.f;
      const int grow = 32 + 16 * hq + (lane >> 2), gf = (grow >> 1) & 7;
#pragma unroll
      for (int s = 0; s < 16; ++s) {
        if ((s & 1) == 0 && s / 2 + 2 < 8) L.unit<false>(s / 2 + 2);
        if (gatew && (s & 1) == 0) {
          const int u = s / 2;
#pragma unroll
          for (int e = 0; e < 2; ++e) {
            const int q = 2 * (lane & 3) + e;
            const u32x4 xv = *(LAS const u32x4*)(lds + L_XR + (u % 3) * XSLOT + grow * 128 + ((q ^ gf) * 16));
            const u32x4 gv = *(LAS const u32x4*)(lds + L_GW + ((u + rot) & 7) * 128 + q * 16);
#pragma unroll
            for (int d = 0; d < 4; ++d) {
              const unsigned xa = xv[d], gb = gv[d];
              ga = __builtin_amdgcn_fdot2_f32_bf16(__builtin_bit_cast(bf2_t, xa), __builtin_bit_cast(bf2_t, gb), ga, false);
            }
          }
        }
        if ((s & 1) == 0) { if (s / 2 + 2 < 8) waitv<5>(); else waitv<0>(); }
        BAR();
      }
      if (gatew) {
        ga += __shfl_xor(ga, 1); ga += __shfl_xor(ga, 2);
        const float g = 1.f / (1.f + __expf(-ga));
        if ((lane & 3) == 0) __hip_atomic_store((GAS float*)(p.ws + WS_G) + tb * 128 + hq * 16 + (lane >> 2), g, RLX_AGENT);
      }
    }
    if (main || halo) {
    STAMP(2);
    const int h0 = 2 * half;
    if (!br) {
      const int T = tb * 4 + tt - 1, hg = 4 * hq + h0;
      auto rq = mkrs(p.ws + WS_QA, FRAG_BYTES), rk = mkrs(p.ws + WS_KA, FRAG_BYTES), rv = mkrs(p.ws + WS_VA, FRAG_BYTES);
      const unsigned o0 = (hg * 64 + T) * 1024 + lane * 16;
      st16<F>(rk, o0, pack8n(ak, 0));
      st16<F>(rk, o0 + 65536, pack8n(ak, 8));
      const unsigned ov = ((hg + ((lane >> 4) & 1)) * 64 + T) * 1024 + hh * 256 + (lane & 15) * 16;
      st16<F>(rv, ov, pack8n(av, 0));
      st16<F>(rv, ov + 512, pack8n(av, 8));
      st16<F>(rq, o0, pack8(aq, 0, QSCALE));
      st16<F>(rq, o0 + 65536, pack8(aq, 8, QSCALE));
    } else {
      if (main || !(wid & 1)) {
        LAS u32x4* kd = (LAS u32x4*)(lds + L_BK + (h0 * 5 + tt) * 1024 + lane * 16);
        kd[0] = pack8n(ak, 0); kd[5 * 64] = pack8n(ak, 8);
      }
      if (main || (wid & 1)) {
        LAS u32x4* vd = (LAS u32x4*)(lds + L_BV + ((h0 + ((lane >> 4) & 1)) * 5 + tt) * 1024 + hh * 256 + (lane & 15) * 16);
        vd[0] = pack8n(av, 0); vd[32] = pack8n(av, 8);
      }
      if (main) {
        LAS u32x4* qd = (LAS u32x4*)(lds + L_BQ + (h0 * 4 + tt - 1) * 1024 + lane * 16);
        qd[0] = pack8(aq, 0, QSCALE); qd[4 * 64] = pack8(aq, 8, QSCALE);
      }
    }
    STAMP(3);
    }
  }
  SETP(0);
  GAS u64* fa1 = (GAS u64*)(p.ws + WS_FA1) + (h2 >> 2) * 16 + (lane & 15);
  if (!br) {
    if (F) {
      WAITV(0);
      BAR();
      if (wid == 0 && lane == 0) set_flag(p.ws, WS_FA1 + (hq * 16 + tb) * 8, tagv);
      if (wid == 15) poll_flag_at(p.ws, (char*)fa1 - p.ws, lane, tagv);
      BAR();
    }
  } else {
    u64 seen = 0;
    WAITL0();
    BAR();
    if (F && wid == 15) seen = __hip_atomic_load(fa1, RLX_AGENT);
    local_unit(p, lds, wid >> 2, wid & 3, 4 * hq + (wid >> 2), tb * 4 + (wid & 3), lane);
    if (F) {
      if (wid == 15 && !__all(seen == tagv)) poll_flag_at(p.ws, (char*)fa1 - p.ws, lane, tagv);
      WAITL0();
      BAR();
    }
  }
}

__device__ __forceinline__ void attn_tile(char* lds, unsigned ka, unsigned va, int off, const bf16x8& qf, f32x16& o, const f32x16& c) {
  bf16x8 kf = lds_b128(lds, ka + off);
  bf16x8 v0 = lds_b128(lds, va + off);
  bf16x8 v1 = lds_b128(lds, va + off + 512);
  f32x16 s = MFMA(kf, qf, c);
  u32x4 p0, p1;
#pragma unroll
  for (int i = 0; i < 4; ++i) p0[i] = cvtpk(__builtin_amdgcn_exp2f(s[2 * i]), __builtin_amdgcn_exp2f(s[2 * i + 1]));
#pragma unroll
  for (int i = 0; i < 4; ++i) p1[i] = cvtpk(__builtin_amdgcn_exp2f(s[8 + 2 * i]), __builtin_amdgcn_exp2f(s[8 + 2 * i + 1]));
  o = MFMA(v0, __builtin_bit_cast(bf16x8, p0), o);
  o = MFMA(v1, __builtin_bit_cast(bf16x8, p1), o);
}

__device__ __forceinline__ void local_unit(const Params& p, char* lds, int hl, int tt, int h, int T, int lane) {
  const int r32 = lane & 31, hh = lane >> 5;
  const bf16x8 q = lds_b128(lds, L_BQ + (hl * 4 + tt) * 1024 + lane * 16);
  const bf16x8 kp = lds_b128(lds, L_BK + (hl * 5 + tt) * 1024 + lane * 16), kc = lds_b128(lds, L_BK + (hl * 5 + tt + 1) * 1024 + lane * 16);
  const unsigned vo = L_BV + (hl * 5 + tt) * 1024 + hh * 256 + (r32 & 15) * 16;
  u32x4 vp = *(LAS const u32x4*)(lds + vo + 512), v0 = *(LAS const u32x4*)(lds + vo + 1024), v1 = *(LAS const u32x4*)(lds + vo + 1536);
  if (r32 >= 16) {
    const unsigned o = (r32 == 16 || r32 == 20) ? 0x3F803F80u : 0u;
    v0 = u32x4{o, o, o, o}; v1 = v0; vp = v0;
  }
  u64 m = 0x5555ull << (18 + r32);
  if (T == 0) m &= 0xFFFFFFFF00000000ull;
  m >>= 4 * hh;
  const unsigned mlo = (unsigned)m, mhi = (unsigned)(m >> 32);
  const float NEG = -1e30f;
  f32x16 cc, cp;
#pragma unroll
  for (int i = 0; i < 16; ++i) {
    const int b = (i & 3) + 8 * (i >> 2);
    cc[i] = ((mhi >> b) & 1u) ? 0.f : NEG;
    cp[i] = (i >= 8 && ((mlo >> b) & 1u)) ? 0.f : NEG;
  }
  f32x16 sc = MFMA(kc, q, cc);
  f32x16 sp = MFMA(kp, q, cp);
  float mx = sc[0];
#pragma unroll
  for (int i = 1; i < 16; ++i) mx = fmaxf(mx, sc[i]);
#pragma unroll
  for (int i = 8; i < 16; ++i) mx = fmaxf(mx, sp[i]);
  {
    auto rr = __builtin_amdgcn_permlane32_swap(__float_as_uint(mx), __float_as_uint(mx), false, false);
    mx = fmaxf(__uint_as_float(rr[0]), __uint_as_float(rr[1]));
  }
  u32x4 p0, p1, pp;
#pragma unroll
  for (int i = 0; i < 4; ++i) {
    p0[i] = cvtpk(__builtin_amdgcn_exp2f(sc[2 * i] - mx), __builtin_amdgcn_exp2f(sc[2 * i + 1] - mx));
    p1[i] = cvtpk(__builtin_amdgcn_exp2f(sc[8 + 2 * i] - mx), __builtin_amdgcn_exp2f(sc[8 + 2 * i + 1] - mx));
    pp[i] = cvtpk(__builtin_amdgcn_exp2f(sp[8 + 2 * i] - mx), __builtin_amdgcn_exp2f(sp[8 + 2 * i + 1] - mx));
  }
  f32x16 o = {};
  o = MFMA(__builtin_bit_cast(bf16x8, v0), __builtin_bit_cast(bf16x8, p0), o);
  o = MFMA(__builtin_bit_cast(bf16x8, v1), __builtin_bit_cast(bf16x8, p1), o);
  o = MFMA(__builtin_bit_cast(bf16x8, vp), __builtin_bit_cast(bf16x8, pp), o);
  __builtin_amdgcn_raw_buffer_store_b128(pack8(o, 0, __builtin_amdgcn_rcpf(o[8])), mkrs(p.ws + WS_OB, FRAG_BYTES), (T * 32 + h) * 1024 + lane * 16, 0, 16);
}

template <bool F>
__device__ __forceinline__ void phase23(const Params& p, char* lds, int wid, int lane, int h, int j, int tb3, int cb, u64 tag, int PH) {
  const unsigned ldsb = (unsigned)(uintptr_t)(LAS char*)lds;
  const int qt = wid & 7, kh = wid >> 3, r32 = lane & 31, hh = lane >> 5;
  LAS float* red = (LAS float*)(lds + L_RED);
  const int w3 = wid & 7, tt = w3 & 1, ct = (w3 >> 1) & 1, kq = w3 >> 2;
  const int T = tb3 * 2 + tt, j3 = tb3 >> 2;
  const int q4 = (lane & 15) >> 2;
  const unsigned wa = (4 * hh + q4) * 128 + ((ct ^ ((q4 >> 1) & 1)) * 64) + ((lane >> 4) & 1) * 32 + (lane & 3) * 8 + kq * 16 * 2048;
  u32x4 of[16];
  unsigned warm0 = 0, warm1 = 0;
  auto load_ob = [&](int i0, int i1) {
    if (F && i0 == 0) poll_flags(p.ws, WS_FB2 + ((tb3 >> 1) * 8 + kq * 4) * 8, 4, lane, tag);
    auto r = mkrs(p.ws + WS_OB, FRAG_BYTES);
#pragma unroll
    for (int i = 0; i < 16; ++i) if (i >= i0 && i < i1) of[i] = ld16<F>(r, (T * 32 + kq * 16 + i) * 1024 + lane * 16);
  };
  if (PH != 3) {
  auto rq = mkrs(p.ws + WS_QA + (size_t)h * 65536, 65536), rk = mkrs(p.ws + WS_KA + (size_t)h * 65536, 65536), rv = mkrs(p.ws + WS_VA + (size_t)h * 65536, 65536);
  u32x4 pc[8]; unsigned pd[8];
  {
    const int t0 = (wid & 3) + ((wid >> 2) & 1) * NT0;
    pc[0] = ld16<F>(kh ? rv : rk, ((t0 + 4 * j) & 63) * 1024 + lane * 16);
    pd[0] = (kh ? L_V : L_K) + t0 * 1024 + lane * 16;
  }
  const bf16x8 qf = __builtin_bit_cast(bf16x8, ld16<F>(rq, (j * 8 + qt) * 1024 + lane * 16));
  {
    const unsigned v = (lane >> 2) == 0 || (lane >> 2) == 4 ? 0x3F803F80u : 0u;
    *(LAS unsigned*)(lds + L_ONE + wid * 256 + lane * 4) = v;
    if (wid == 0 && lane < 2) *(LAS unsigned*)(lds + L_MISC + lane * 4) = 0u;
  }
  unsigned kaddr = L_K + kh * NT0 * 1024 + lane * 16, vaddr, vstep;
  if (r32 < 16) { vaddr = L_V + kh * NT0 * 1024 + hh * 256 + r32 * 16; vstep = 2048; }
  else { vaddr = L_ONE + hh * 256 + ((r32 >= 20 && r32 < 28) ? 64 : 0) + ((r32 == 16 || r32 == 20) ? 0 : 16); vstep = 0; }
  const unsigned kaddr0 = kaddr, vaddr0 = vaddr;
  f32x16 o = {};
  const f32x16 zero = {};
  STAMP(9);
  *(LAS u32x4*)(lds + pd[0]) = pc[0];
  SB();
#pragma unroll
  for (int n = 1; n < 8; ++n) {
    const int i = (n - 1) * 16 + wid, ti = i >> 1;
    const int t = ti < NT0 - 4 ? 4 + ti : 8 + ti;
    pc[n] = ld16<F>((wid & 1) ? rv : rk, ((t + 4 * j) & 63) * 1024 + lane * 16);
    pd[n] = ((wid & 1) ? L_V : L_K) + t * 1024 + lane * 16;
  }
  WAITL0();
  BAR();
  STAMP(10);
  SETP(3); attn_tile(lds, kaddr, vaddr, 0, qf, o, zero);
  *(LAS u32x4*)(lds + pd[1]) = pc[1]; *(LAS u32x4*)(lds + pd[2]) = pc[2];
  SETP(2); attn_tile(lds, kaddr, vaddr, 1024, qf, o, zero);
  *(LAS u32x4*)(lds + pd[3]) = pc[3]; *(LAS u32x4*)(lds + pd[4]) = pc[4];
  kaddr += 2048; vaddr += vstep;
  SETP(1); attn_tile(lds, kaddr, vaddr, 0, qf, o, zero);
  *(LAS u32x4*)(lds + pd[5]) = pc[5]; *(LAS u32x4*)(lds + pd[6]) = pc[6];
  SETP(0); attn_tile(lds, kaddr, vaddr, 1024, qf, o, zero);
  *(LAS u32x4*)(lds + pd[7]) = pc[7];
  kaddr += 2048; vaddr += vstep;
  if (F) WAITV(0);
  WAITL0();
  STAMP(11);
  BAR();
  if (F && wid == 0 && lane == 0) {
    const int b = blockIdx.x, xcd = b & 7, idx = b >> 3;
    if ((idx >> 3) & 1) set_flag(p.ws, WS_FB2 + (((xcd & 1) * 8 + (idx & 7)) * 8 + (xcd >> 1) * 2 + (idx >> 4)) * 8, tag);
  }
  if (F && kh == 1) {
    const char* a0 = p.woA + (size_t)(w3 * 64 + lane) * 1024 + cb * 128;
    const char* a1 = p.woB + (size_t)(w3 * 64 + lane) * 1024 + cb * 128;
    asm volatile("global_load_dword %0, %2, off\n\tglobal_load_dword %1, %3, off" : "=&v"(warm0), "=&v"(warm1) : "v"(a0), "v"(a1) : "memory");
  }
  SETP(3);
  const int nit = kh ? (60 - NT0) / 2 : (NT0 - 4) / 2;
#pragma unroll 1
  for (int it = nit; it > 2; --it) {
    if (it == 6) SETP(2);
    if (it == 3) SETP(1);
    attn_tile(lds, kaddr, vaddr, 0, qf, o, zero);
    attn_tile(lds, kaddr, vaddr, 1024, qf, o, zero);
    kaddr += 2048; vaddr += vstep;
  }
  if (F && kh == 1) load_ob(0, 8);
  SB();
  attn_tile(lds, kaddr, vaddr, 0, qf, o, zero);
  SB();
  attn_tile(lds, kaddr, vaddr, 1024, qf, o, zero);
  SB();
  kaddr += 2048; vaddr += vstep;
  SETP(0);
  attn_tile(lds, kaddr, vaddr, 0, qf, o, zero);
  SB();
  attn_tile(lds, kaddr, vaddr, 1024, qf, o, zero);
  SB();
  STAMP(13);
  auto rO = mkrs(p.ws + WS_OA, FRAG_BYTES);
  const unsigned oo = ((j * 8 + qt) * 32 + h) * 1024 + lane * 16;
  if (kh == 1) {
#pragma unroll
    for (int i = 0; i < 9; ++i) red[(qt * 9 + i) * 64 + lane] = o[i];
  }
  WAITL0();
  BAR();
  if (kh == 0) {
    bool ok = true;
#pragma unroll
    for (int i = 0; i < 9; ++i) { o[i] += red[(qt * 9 + i) * 64 + lane]; ok = ok && (fabsf(o[i]) <= 3.0e38f); }
    ok = ok && (o[8] >= 8.67e-19f);
    if (!__all(ok)) { if (lane == 0) *(LAS unsigned*)(lds + L_MISC + 4) = 1u; }
  }
  WAITL0();
  BAR();
  if (*(LAS volatile unsigned*)(lds + L_MISC + 4)) {
    const int nt = kh ? 64 - NT0 : NT0;
    float mx = -3.0e38f;
    for (int t = 0; t < nt; ++t) {
      f32x16 s = MFMA(lds_b128(lds, kaddr0 + t * 1024), qf, zero);
#pragma unroll
      for (int i = 0; i < 16; ++i) mx = fmaxf(mx, s[i]);
    }
    { auto rr = __builtin_amdgcn_permlane32_swap(__float_as_uint(mx), __float_as_uint(mx), false, false);
      mx = fmaxf(__uint_as_float(rr[0]), __uint_as_float(rr[1])); }
    red[wid * 64 + lane] = mx;
    WAITL0(); BAR();
    mx = fmaxf(mx, red[(wid ^ 8) * 64 + lane]);
    WAITL0(); BAR();
    f32x16 c;
#pragma unroll
    for (int i = 0; i < 16; ++i) { c[i] = -mx; o[i] = 0.f; }
    unsigned va = vaddr0;
    for (int t = 0; t < nt; ++t) { attn_tile(lds, kaddr0 + t * 1024, va, 0, qf, o, c); va += vstep >> 1; }
    if (kh == 1) {
#pragma unroll
      for (int i = 0; i < 9; ++i) red[(qt * 9 + i) * 64 + lane] = o[i];
    }
    WAITL0(); BAR();
    if (kh == 0) {
#pragma unroll
      for (int i = 0; i < 9; ++i) o[i] += red[(qt * 9 + i) * 64 + lane];
    }
    WAITL0(); BAR();
    if (F && kh == 1) load_ob(0, 8);
  }
  if (kh == 0) {
    st16<F>(rO, oo, pack8(o, 0, __builtin_amdgcn_rcpf(o[8])));
    if (F) {
      WAITV(0);
      unsigned old = 0;
      if (lane == 0) old = __hip_atomic_fetch_add((LAS unsigned*)(lds + L_MISC), 1u, __ATOMIC_RELAXED, __HIP_MEMORY_SCOPE_WORKGROUP);
      if (lane == 0 && old == 7) set_flag(p.ws, WS_FA2 + (j * 32 + h) * 8, tag);
    }
  }
  STAMP(14);
  }
  if (PH == 2) return;
  f32x16 acc = {}, accA = {};
  float gate = 0.f;
  if (kh == 1) {
    const int row = lane >> 3, pos = (lane & 7) ^ (((row >> 1) & 1) << 2);
    const size_t so = (size_t)(w3 * 8 + row) * 1024 + cb * 128 + pos * 16;
#pragma unroll
    for (int n = 0; n < 8; ++n) glds16(p.woB + so + n * 65536, ldsb + L_WB + (n * 8 + w3) * 1024);
    if (!F) load_ob(0, 8);
    load_ob(8, 16);
#pragma unroll
    for (int n = 0; n < 8; ++n) glds16(p.woA + so + n * 65536, ldsb + L_WA + (n * 8 + w3) * 1024);
    if (F) WAITV(16); else WAITV(24);
    asm volatile("" :: "v"(warm0), "v"(warm1));
    STAMP(17);
  }
  BAR();
  if (kh == 1) {
    STAMP(18);
#pragma unroll
    for (int i = 0; i < 16; ++i) acc = MFMA(lds_tr2(lds, L_WB + wa + i * 2048, 1024), __builtin_bit_cast(bf16x8, of[i]), acc);
    STAMP(19);
    if (F) poll_flags(p.ws, WS_FA2 + (j3 * 32 + kq * 16) * 8, 16, lane, tag);
    gate = __hip_atomic_load((GAS float*)(p.ws + WS_G) + T * 32 + r32, RLX_AGENT);
    {
      auto r = mkrs(p.ws + WS_OA, FRAG_BYTES);
#pragma unroll
      for (int i = 0; i < 16; ++i) of[i] = ld16<F>(r, (T * 32 + kq * 16 + i) * 1024 + lane * 16);
    }
    WAITV(16);
    STAMP(20);
  }
  BAR();
  LAS float* red3 = (LAS float*)(lds + L_RED3) + (wid & 3) * 1024;
  if (kh == 1) {
#pragma unroll
    for (int i = 0; i < 16; ++i) accA = MFMA(lds_tr2(lds, L_WA + wa + i * 2048, 1024), __builtin_bit_cast(bf16x8, of[i]), accA);
#pragma unroll
    for (int i = 0; i < 16; ++i) acc[i] = gate * accA[i] + (1.f - gate) * acc[i];
    STAMP(21);
    if (kq == 1) {
#pragma unroll
      for (int i = 0; i < 16; ++i) red3[i * 64 + lane] = acc[i];
    }
    WAITL0();
  }
  BAR();
  if (kh == 1 && kq == 0) {
#pragma unroll
    for (int i = 0; i < 16; ++i) acc[i] += red3[i * 64 + lane];
    unsigned w[8];
#pragma unroll
    for (int i = 0; i < 8; ++i) w[i] = cvtpk(acc[2 * i], acc[2 * i + 1]);
    char* dst = p.out + (size_t)(T * 32 + (lane & 31)) * 1024 + cb * 128 + ct * 64 + hh * 16;
#pragma unroll
    for (int g = 0; g < 4; g += 2) {
      auto a = __builtin_amdgcn_permlane32_swap(w[2 * g], w[2 * g + 2], false, false);
      auto b = __builtin_amdgcn_permlane32_swap(w[2 * g + 1], w[2 * g + 3], false, false);
      *(GAS u32x4*)(dst + g * 16) = u32x4{a[0], b[0], a[1], b[1]};
    }
  }
}

template <int PH>
__global__ __launch_bounds__(1024) void gated_mix_kernel(Params p) {
  extern __shared__ __attribute__((aligned(16))) char lds[];
  const int wid = __builtin_amdgcn_readfirstlane(threadIdx.x >> 6);
  const int lane = __builtin_amdgcn_mbcnt_hi(~0u, __builtin_amdgcn_mbcnt_lo(~0u, 0u));
  const int b = blockIdx.x, xcd = b & 7, idx = b >> 3;
  u64 tag = 0;
  if (PH == 0) tag = __hip_atomic_load((GAS u64*)(p.ws + WS_NONCE), RLX_AGENT) + 1;
  STAMP(0 + (PH >= 2 ? (PH - 1) * 8 : 0));
  if (PH == 0 || PH == 1) phase1<PH == 0>(p, lds, wid, lane, (idx >> 3) & 1, (xcd >> 1) * 2 + (idx >> 4), (xcd & 1) * 8 + (idx & 7), ((idx & 7) + 2 * (idx >> 3)) & 7, xcd * 4 + (idx >> 3), tag);
  if (PH == 0 || PH == 1) STAMP(4);
  tag = ((u64)__builtin_amdgcn_readfirstlane((unsigned)(tag >> 32)) << 32) | (unsigned)__builtin_amdgcn_readfirstlane((unsigned)tag);
  if (PH != 1) phase23<PH == 0>(p, lds, wid, lane, xcd * 4 + (idx >> 3), idx & 7, xcd * 4 + (idx >> 3), idx & 7, tag, PH);
  STAMP(23);
  if (PH == 0 && b == 0 && wid == 0 && lane == 0) set_flag(p.ws, WS_NONCE, tag);
}

extern "C" void kernel_launch(void* const* d_in, const int* in_sizes, int n_in, void* d_out, int out_size, void* d_ws, size_t ws_size,
                              hipStream_t stream) {
  Params p;
  p.x = (const char*)d_in[0]; p.wA = (const char*)d_in[1]; p.woA = (const char*)d_in[2];
  p.wB = (const char*)d_in[4]; p.woB = (const char*)d_in[5]; p.gw = (const char*)d_in[6];
  p.out = (char*)d_out; p.ws = (char*)d_ws;
  (void)hipFuncSetAttribute((const void*)gated_mix_kernel<0>, hipFuncAttributeMaxDynamicSharedMemorySize, LDS_TOTAL);
  gated_mix_kernel<0><<<256, 1024, LDS_TOTAL, stream>>>(p);
}
```

```cpp
#define DIAG_A 0
#define DIAG_B 1
#define DIAG_W 0
#define DIAG_K 8
#define DIAG_BLK 0
#include <hip/hip_runtime.h>
#include <hip/hip_bf16.h>
#include <stdint.h>
#include <type_traits>

#define FUSE 1

typedef unsigned long long u64;
using f32x16 = __attribute__((ext_vector_type(16))) float;
using bf16x8 = __attribute__((ext_vector_type(8))) short;
using s16x4 = __attribute__((ext_vector_type(4))) short;
using u32x4 = __attribute__((ext_vector_type(4))) unsigned;
typedef __bf16 bf2_t __attribute__((ext_vector_type(2)));
typedef float f2_t __attribute__((ext_vector_type(2)));
#define GAS __attribute__((address_space(1)))
#define LAS __attribute__((address_space(3)))
#define CAS __attribute__((address_space(4)))
#define MFMA(a, b, c) __builtin_amdgcn_mfma_f32_32x32x16_bf16((a), (b), (c), 0, 0, 0)
#define MFMA1(a, b, c) MFMA(a, b, c)
#define RLX_AGENT __ATOMIC_RELAXED, __HIP_MEMORY_SCOPE_AGENT
#define BAR() asm volatile("s_barrier" ::: "memory")
#define SBAR_STAGE() BAR()
#define WAITV(n) asm volatile("s_waitcnt vmcnt(" #n ")" ::: "memory")
#define WAITL0() asm volatile("s_waitcnt lgkmcnt(0)" ::: "memory")
#define SETP(x) __builtin_amdgcn_s_setprio(x)

constexpr size_t WS_NONCE = 0;
constexpr size_t WS_FA1 = 4096, WS_FB2 = 12288, WS_FA2 = 16384, WS_TMO = 20480;
constexpr size_t WS_G = 32768;
constexpr size_t MB = 1u << 20;
constexpr size_t WS_QA = 1 * MB, WS_KA = 3 * MB, WS_VA = 5 * MB, WS_OA = 7 * MB, WS_OB = 9 * MB;
constexpr unsigned FRAG_BYTES = 2u << 20;
constexpr int LDS_TOTAL = 160 * 1024;
constexpr int L_XR = 0, XSLOT = 160 * 128, L_WR = 3 * XSLOT, WSLOT = 3 * 64 * 128, L_GW = L_WR + 3 * WSLOT;
constexpr int L_BK = 0, L_BV = 20480, L_BQ = 40960;
static_assert(L_GW + 1024 <= 160 * 1024, "lds");
constexpr int L_K = 0, L_V = 65536, L_ONE = 131072, L_RED = L_ONE + 4096, L_MISC = L_RED + 8 * 9 * 256;
constexpr int L_WB = 0, L_WA = 65536, L_RED3 = 131072;
static_assert(L_MISC + 256 <= LDS_TOTAL, "lds");
constexpr float QSCALE = 0.25f * 1.4426950408889634f;
constexpr int NT0 = 32;
constexpr unsigned SPIN_CAP = 1u << 20;

struct Params {
  const char* x; const char* wA; const char* woA; const char* wB; const char* woB; const char* gw; char* out; char* ws;
};

__device__ __forceinline__ unsigned cvtpk(float lo, float hi) {
  f2_t v = {lo, hi};
  return __builtin_bit_cast(unsigned, __builtin_convertvector(v, bf2_t));
}
__device__ __forceinline__ u32x4 pack8(const f32x16& a, int base, float s) {
  u32x4 r;
#pragma unroll
  for (int i = 0; i < 4; ++i) r[i] = cvtpk(a[base + 2 * i] * s, a[base + 2 * i + 1] * s);
  return r;
}
__device__ __forceinline__ u32x4 pack8n(const f32x16& a, int base) {
  u32x4 r;
#pragma unroll
  for (int i = 0; i < 4; ++i) r[i] = cvtpk(a[base + 2 * i], a[base + 2 * i + 1]);
  return r;
}
template <bool F> __device__ __forceinline__ void st16(__amdgpu_buffer_rsrc_t r, unsigned off, u32x4 v) {
  __builtin_amdgcn_raw_buffer_store_b128(v, r, off, 0, F ? 16 : 0);
}
template <bool F> __device__ __forceinline__ u32x4 ld16(__amdgpu_buffer_rsrc_t r, unsigned off) {
  return __builtin_amdgcn_raw_buffer_load_b128(r, off, 0, F ? 16 : 0);
}
__device__ __forceinline__ __amdgpu_buffer_rsrc_t mkrs(const void* p, unsigned bytes) {
  return __builtin_amdgcn_make_buffer_rsrc((void*)p, 0, bytes, 0x00020000);
}
__device__ __forceinline__ bf16x8 lds_b128(char* lds, unsigned a) { return *(LAS const bf16x8*)(lds + a); }
__device__ __forceinline__ bf16x8 lds_tr2(char* lds, unsigned a, unsigned second) {
  s16x4 lo = __builtin_amdgcn_ds_read_tr16_b64_v4i16((LAS s16x4*)(lds + a));
  s16x4 hi = __builtin_amdgcn_ds_read_tr16_b64_v4i16((LAS s16x4*)(lds + a + second));
  return __builtin_shufflevector(lo, hi, 0, 1, 2, 3, 4, 5, 6, 7);
}
__device__ __forceinline__ void glds16(const void* g, unsigned dst) {
  unsigned sv;
  asm volatile("s_mov_b32 %0, m0\n\ts_mov_b32 m0, %2\n\ts_nop 0\n\tglobal_load_lds_dwordx4 %1, off\n\ts_mov_b32 m0, %0" : "=&s"(sv) : "v"(g), "s"(dst) : "memory");
}
__device__ __forceinline__ void glds16s(const void* sbase, unsigned voff, unsigned dst) {
  unsigned sv;
  asm volatile("s_mov_b32 %0, m0\n\ts_mov_b32 m0, %3\n\ts_nop 0\n\tglobal_load_lds_dwordx4 %1, %2\n\ts_mov_b32 m0, %0" : "=&s"(sv) : "v"(voff), "s"(sbase), "s"(dst) : "memory");
}
#define SB() __builtin_amdgcn_sched_barrier(0)
#define OPAQUE(x) asm volatile("" : "+v"(x))
#define STAMP(i) do { if (((i) == DIAG_A || (i) == DIAG_B) && wid == DIAG_W && lane == 0) *(LAS volatile u64*)(lds + 163808 + ((i) == DIAG_A ? 0 : 8)) = __builtin_amdgcn_s_memrealtime(); } while (0)
#define CSTAMP(i)
__device__ __forceinline__ void poll_flag_at(char* ws, size_t off, int lane, u64 tag) {
  GAS u64* f = (GAS u64*)(ws + off);
  unsigned spins = 0;
  while (true) {
    u64 v = __hip_atomic_load(f, RLX_AGENT);
    if (__all(v == tag)) break;
    __builtin_amdgcn_s_sleep(1);
    if (++spins > SPIN_CAP) { if (lane == 0) __hip_atomic_store((GAS unsigned*)(ws + WS_TMO), 1u, RLX_AGENT); break; }
  }
}
__device__ __forceinline__ void poll_flags(char* ws, size_t off, int n, int lane, u64 tag) { poll_flag_at(ws, off + (lane < n ? lane : 0) * 8, lane, tag); }
__device__ __forceinline__ void set_flag(char* ws, size_t off, u64 tag) { __hip_atomic_store((GAS u64*)(ws + off), tag, RLX_AGENT); }

template <int N> __device__ __forceinline__ void waitv() { asm volatile("s_waitcnt vmcnt(%0)" :: "n"(N) : "memory"); }

struct P1Loader {
  const char* xs; const char* wsrc; unsigned xo, wo, ldsb; int lw, rot, halo_fix;
  __device__ __forceinline__ void init(const Params& p, unsigned ldsb_, int lw_, int lane, int br, int hq, int tb, int rot_) {
    ldsb = ldsb_; lw = lw_; rot = rot_;
    const int qs = (lane & 7) ^ ((lane >> 4) & 3) ^ (4 * (lw & 1));
    xs = p.x + (ptrdiff_t)(tb * 128 - 32) * 1024;
    xo = (lane >> 3) * 1024 + qs * 16;
    halo_fix = tb == 0 ? 32 * 1024 : 0;
    wsrc = (br ? p.wB : p.wA) + (size_t)(8 * lw) * 3072 + hq * 128;
    wo = (lane >> 3) * 3072 + (((lane & 7) ^ (((lane >> 4) & 1) << 2)) * 16);
  }
  template <bool HALO> __device__ __forceinline__ void unit(int u) {
    const int ku = (u + rot) & 7;
    const unsigned xd = ldsb + L_XR + (u % 3) * XSLOT, wd = ldsb + L_WR + (u % 3) * WSLOT + lw * 1024;
    glds16s(xs + (4 + lw) * 8192 + ku * 128, xo, xd + (4 + lw) * 1024);
    glds16s(wsrc + (size_t)ku * 64 * 3072, wo, wd);
    glds16s(xs + (12 + lw) * 8192 + ku * 128, xo, xd + (12 + lw) * 1024);
    glds16s(wsrc + (size_t)ku * 64 * 3072 + 1024, wo, wd + 8192);
    glds16s(wsrc + (size_t)ku * 64 * 3072 + 2048, wo, wd + 16384);
    if (HALO) glds16s(xs + lw * 8192 + ku * 128 + halo_fix, xo, xd + lw * 1024);
  }
};

__device__ __forceinline__ void local_unit(const Params& p, char* lds, int hl, int tt, int h, int T, int lane);

template <bool F>
__device__ __forceinline__ void phase1(const Params& p, char* lds, int wid, int lane, int br, int hq, int tb, int rot, int h2, u64 tagv) {
  const unsigned ldsb = (unsigned)(uintptr_t)(LAS char*)lds;
  const int r32 = lane & 31, hh = lane >> 5, q4 = (lane & 15) >> 2;
  const bool halo = br && wid >= 8 && wid < 12;
  {
    const bool main = wid < 8;
    const int tt = main ? (wid & 3) + 1 : 0, half = main ? wid >> 2 : (wid >> 1) & 1;
    const int row = 32 * tt + r32, f = (row >> 1) & 7;
    unsigned xa0 = L_XR + row * 128 + (((hh ^ (f & 1)) | (f & 6)) * 16);
    unsigned wa0 = L_WR + (8 * hh + q4) * 128 + ((half ^ ((q4 >> 1) & 1)) * 64) + ((lane >> 4) & 1) * 32 + (lane & 3) * 8;
    OPAQUE(wa0);
    auto ldx = [&](int ks) { return lds_b128(lds, (xa0 ^ ((ks & 3) << 5)) + ((ks >> 2) % 3) * XSLOT); };
    auto ldw = [&](int ks, int c) { return lds_tr2(lds, wa0 + ((ks >> 2) % 3) * WSLOT + (ks & 3) * 2048 + c * 8192, 512); };
    f32x16 aq = {}, ak = {}, av = {};
    if (main) {
      BAR();
      STAMP(1);
      bf16x8 xf[3], wq[3], wk[3], wv[3];
      xf[0] = ldx(0); wq[0] = ldw(0, 0); wk[0] = ldw(0, 1); wv[0] = ldw(0, 2);
      xf[1] = ldx(1); wq[1] = ldw(1, 0); wk[1] = ldw(1, 1); wv[1] = ldw(1, 2);
#pragma unroll
      for (int ks = 0; ks < 32; ++ks) {
        const int c = ks % 3, n = (ks + 2) % 3;
        SB();
        aq = MFMA1(wq[c], xf[c], aq);
        SB();
        if (ks < 30) { xf[n] = ldx(ks + 2); wq[n] = ldw(ks + 2, 0); }
        SB();
        ak = MFMA1(wk[c], xf[c], ak);
        SB();
        if (ks < 30) wk[n] = ldw(ks + 2, 1);
        SB();
        av = MFMA1(xf[c], wv[c], av);
        SB();
        if (ks < 30) wv[n] = ldw(ks + 2, 2);
        if (ks & 1) BAR();
      }
    } else if (halo) {
      SETP(3);
      P1Loader L; L.init(p, ldsb, wid - 8, lane, br, hq, tb, rot);
      L.unit<true>(0); L.unit<true>(1);
      waitv<6>();
      BAR();
      auto halo_loop = [&](auto ISV) {
        constexpr bool isv = decltype(ISV)::value;
        wa0 += isv ? 16384 : 8192;
        bf16x8 xf[3], wf[3];
        f32x16 a = {};
        xf[0] = ldx(0); wf[0] = ldw(0, 0); xf[1] = ldx(1); wf[1] = ldw(1, 0);
#pragma unroll
        for (int ks = 0; ks < 32; ++ks) {
          const int c = ks % 3, n = (ks + 2) % 3;
          if ((ks & 3) == 0 && ks / 4 + 2 < 8) L.unit<true>(ks / 4 + 2);
          SB();
          if (isv) a = MFMA1(xf[c], wf[c], a); else a = MFMA1(wf[c], xf[c], a);
          SB();
          if (ks < 30) { xf[n] = ldx(ks + 2); wf[n] = ldw(ks + 2, 0); }
          if ((ks & 3) == 1) { if (ks / 4 + 2 < 8) waitv<6>(); else waitv<0>(); }
          if (ks & 1) BAR();
        }
        return a;
      };
      if (wid & 1) av = halo_loop(std::true_type{}); else ak = halo_loop(std::false_type{});
    } else {
      SETP(3);
      P1Loader L; L.init(p, ldsb, wid - 8, lane, br, hq, tb, rot);
      const bool gatew = !br && wid == 8;
      if (gatew) glds16(p.gw + lane * 16, ldsb + L_GW);
      L.unit<false>(0); L.unit<false>(1);
      waitv<5>();
      BAR();
      float ga = 0.f;
      const int grow = 32 + 16 * hq + (lane >> 2), gf = (grow >> 1) & 7;
#pragma unroll
      for (int s = 0; s < 16; ++s) {
        if ((s & 1) == 0 && s / 2 + 2 < 8) L.unit<false>(s / 2 + 2);
        if (gatew && (s & 1) == 0) {
          const int u = s / 2;
#pragma unroll
          for (int e = 0; e < 2; ++e) {
            const int q = 2 * (lane & 3) + e;
            const u32x4 xv = *(LAS const u32x4*)(lds + L_XR + (u % 3) * XSLOT + grow * 128 + ((q ^ gf) * 16));
            const u32x4 gv = *(LAS const u32x4*)(lds + L_GW + ((u + rot) & 7) * 128 + q * 16);
#pragma unroll
            for (int d = 0; d < 4; ++d) {
              const unsigned xa = xv[d], gb = gv[d];
              ga = __builtin_amdgcn_fdot2_f32_bf16(__builtin_bit_cast(bf2_t, xa), __builtin_bit_cast(bf2_t, gb), ga, false);
            }
          }
        }
        if ((s & 1) == 0) { if (s / 2 + 2 < 8) waitv<5>(); else waitv<0>(); }
        BAR();
      }
      if (gatew) {
        ga += __shfl_xor(ga, 1); ga += __shfl_xor(ga, 2);
        const float g = 1.f / (1.f + __expf(-ga));
        if ((lane & 3) == 0) __hip_atomic_store((GAS float*)(p.ws + WS_G) + tb * 128 + hq * 16 + (lane >> 2), g, RLX_AGENT);
      }
    }
    if (main || halo) {
    STAMP(2);
    const int h0 = 2 * half;
    if (!br) {
      const int T = tb * 4 + tt - 1, hg = 4 * hq + h0;
      auto rq = mkrs(p.ws + WS_QA, FRAG_BYTES), rk = mkrs(p.ws + WS_KA, FRAG_BYTES), rv = mkrs(p.ws + WS_VA, FRAG_BYTES);
      const unsigned o0 = (hg * 64 + T) * 1024 + lane * 16;
      st16<F>(rk, o0, pack8n(ak, 0));
      st16<F>(rk, o0 + 65536, pack8n(ak, 8));
      const unsigned ov = ((hg + ((lane >> 4) & 1)) * 64 + T) * 1024 + hh * 256 + (lane & 15) * 16;
      st16<F>(rv, ov, pack8n(av, 0));
      st16<F>(rv, ov + 512, pack8n(av, 8));
      st16<F>(rq, o0, pack8(aq, 0, QSCALE));
      st16<F>(rq, o0 + 65536, pack8(aq, 8, QSCALE));
    } else {
      if (main || !(wid & 1)) {
        LAS u32x4* kd = (LAS u32x4*)(lds + L_BK + (h0 * 5 + tt) * 1024 + lane * 16);
        kd[0] = pack8n(ak, 0); kd[5 * 64] = pack8n(ak, 8);
      }
      if (main || (wid & 1)) {
        LAS u32x4* vd = (LAS u32x4*)(lds + L_BV + ((h0 + ((lane >> 4) & 1)) * 5 + tt) * 1024 + hh * 256 + (lane & 15) * 16);
        vd[0] = pack8n(av, 0); vd[32] = pack8n(av, 8);
      }
      if (main) {
        LAS u32x4* qd = (LAS u32x4*)(lds + L_BQ + (h0 * 4 + tt - 1) * 1024 + lane * 16);
        qd[0] = pack8(aq, 0, QSCALE); qd[4 * 64] = pack8(aq, 8, QSCALE);
      }
    }
    STAMP(3);
    }
  }
  SETP(0);
  GAS u64* fa1 = (GAS u64*)(p.ws + WS_FA1) + (h2 >> 2) * 16 + (lane & 15);
  if (!br) {
    if (F) {
      WAITV(0);
      BAR();
      if (wid == 0 && lane == 0) set_flag(p.ws, WS_FA1 + (hq * 16 + tb) * 8, tagv);
      if (wid == 15) poll_flag_at(p.ws, (char*)fa1 - p.ws, lane, tagv);
      BAR();
    }
  } else {
    u64 seen = 0;
    WAITL0();
    BAR();
    if (F && wid == 15) seen = __hip_atomic_load(fa1, RLX_AGENT);
    local_unit(p, lds, wid >> 2, wid & 3, 4 * hq + (wid >> 2), tb * 4 + (wid & 3), lane);
    if (F) {
      if (wid == 15 && !__all(seen == tagv)) poll_flag_at(p.ws, (char*)fa1 - p.ws, lane, tagv);
      WAITL0();
      BAR();
    }
  }
}

__device__ __forceinline__ void attn_tile(char* lds, unsigned ka, unsigned va, int off, const bf16x8& qf, f32x16& o, const f32x16& c) {
  bf16x8 kf = lds_b128(lds, ka + off);
  bf16x8 v0 = lds_b128(lds, va + off);
  bf16x8 v1 = lds_b128(lds, va + off + 512);
  f32x16 s = MFMA(kf, qf, c);
  u32x4 p0, p1;
#pragma unroll
  for (int i = 0; i < 4; ++i) p0[i] = cvtpk(__builtin_amdgcn_exp2f(s[2 * i]), __builtin_amdgcn_exp2f(s[2 * i + 1]));
#pragma unroll
  for (int i = 0; i < 4; ++i) p1[i] = cvtpk(__builtin_amdgcn_exp2f(s[8 + 2 * i]), __builtin_amdgcn_exp2f(s[8 + 2 * i + 1]));
  o = MFMA(v0, __builtin_bit_cast(bf16x8, p0), o);
  o = MFMA(v1, __builtin_bit_cast(bf16x8, p1), o);
}

__device__ __forceinline__ void local_unit(const Params& p, char* lds, int hl, int tt, int h, int T, int lane) {
  const int r32 = lane & 31, hh = lane >> 5;
  const bf16x8 q = lds_b128(lds, L_BQ + (hl * 4 + tt) * 1024 + lane * 16);
  const bf16x8 kp = lds_b128(lds, L_BK + (hl * 5 + tt) * 1024 + lane * 16), kc = lds_b128(lds, L_BK + (hl * 5 + tt + 1) * 1024 + lane * 16);
  const unsigned vo = L_BV + (hl * 5 + tt) * 1024 + hh * 256 + (r32 & 15) * 16;
  u32x4 vp = *(LAS const u32x4*)(lds + vo + 512), v0 = *(LAS const u32x4*)(lds + vo + 1024), v1 = *(LAS const u32x4*)(lds + vo + 1536);
  if (r32 >= 16) {
    const unsigned o = (r32 == 16 || r32 == 20) ? 0x3F803F80u : 0u;
    v0 = u32x4{o, o, o, o}; v1 = v0; vp = v0;
  }
  u64 m = 0x5555ull << (18 + r32);
  if (T == 0) m &= 0xFFFFFFFF00000000ull;
  m >>= 4 * hh;
  const unsigned mlo = (unsigned)m, mhi = (unsigned)(m >> 32);
  const float NEG = -1e30f;
  f32x16 cc, cp;
#pragma unroll
  for (int i = 0; i < 16; ++i) {
    const int b = (i & 3) + 8 * (i >> 2);
    cc[i] = ((mhi >> b) & 1u) ? 0.f : NEG;
    cp[i] = (i >= 8 && ((mlo >> b) & 1u)) ? 0.f : NEG;
  }
  f32x16 sc = MFMA(kc, q, cc);
  f32x16 sp = MFMA(kp, q, cp);
  float mx = sc[0];
#pragma unroll
  for (int i = 1; i < 16; ++i) mx = fmaxf(mx, sc[i]);
#pragma unroll
  for (int i = 8; i < 16; ++i) mx = fmaxf(mx, sp[i]);
  {
    auto rr = __builtin_amdgcn_permlane32_swap(__float_as_uint(mx), __float_as_uint(mx), false, false);
    mx = fmaxf(__uint_as_float(rr[0]), __uint_as_float(rr[1]));
  }
  u32x4 p0, p1, pp;
#pragma unroll
  for (int i = 0; i < 4; ++i) {
    p0[i] = cvtpk(__builtin_amdgcn_exp2f(sc[2 * i] - mx), __builtin_amdgcn_exp2f(sc[2 * i + 1] - mx));
    p1[i] = cvtpk(__builtin_amdgcn_exp2f(sc[8 + 2 * i] - mx), __builtin_amdgcn_exp2f(sc[8 + 2 * i + 1] - mx));
    pp[i] = cvtpk(__builtin_amdgcn_exp2f(sp[8 + 2 * i] - mx), __builtin_amdgcn_exp2f(sp[8 + 2 * i + 1] - mx));
  }
  f32x16 o = {};
  o = MFMA(__builtin_bit_cast(bf16x8, v0), __builtin_bit_cast(bf16x8, p0), o);
  o = MFMA(__builtin_bit_cast(bf16x8, v1), __builtin_bit_cast(bf16x8, p1), o);
  o = MFMA(__builtin_bit_cast(bf16x8, vp), __builtin_bit_cast(bf16x8, pp), o);
  __builtin_amdgcn_raw_buffer_store_b128(pack8(o, 0, __builtin_amdgcn_rcpf(o[8])), mkrs(p.ws + WS_OB, FRAG_BYTES), (T * 32 + h) * 1024 + lane * 16, 0, 16);
}

template <bool F>
__device__ __forceinline__ void phase23(const Params& p, char* lds, int wid, int lane, int h, int j, int tb3, int cb, u64 tag, int PH) {
  const unsigned ldsb = (unsigned)(uintptr_t)(LAS char*)lds;
  const int qt = wid & 7, kh = wid >> 3, r32 = lane & 31, hh = lane >> 5;
  LAS float* red = (LAS float*)(lds + L_RED);
  const int w3 = wid & 7, tt = w3 & 1, ct = (w3 >> 1) & 1, kq = w3 >> 2;
  const int T = tb3 * 2 + tt, j3 = tb3 >> 2;
  const int q4 = (lane & 15) >> 2;
  const unsigned wa = (4 * hh + q4) * 128 + ((ct ^ ((q4 >> 1) & 1)) * 64) + ((lane >> 4) & 1) * 32 + (lane & 3) * 8 + kq * 16 * 2048;
  u32x4 of[16];
  unsigned warm0 = 0, warm1 = 0;
  auto load_ob = [&](int i0, int i1) {
    if (F && i0 == 0) poll_flags(p.ws, WS_FB2 + ((tb3 >> 1) * 8 + kq * 4) * 8, 4, lane, tag);
    auto r = mkrs(p.ws + WS_OB, FRAG_BYTES);
#pragma unroll
    for (int i = 0; i < 16; ++i) if (i >= i0 && i < i1) of[i] = ld16<F>(r, (T * 32 + kq * 16 + i) * 1024 + lane * 16);
  };
  if (PH != 3) {
  auto rq = mkrs(p.ws + WS_QA + (size_t)h * 65536, 65536), rk = mkrs(p.ws + WS_KA + (size_t)h * 65536, 65536), rv = mkrs(p.ws + WS_VA + (size_t)h * 65536, 65536);
  u32x4 pc[8]; unsigned pd[8];
  {
    const int t0 = (wid & 3) + ((wid >> 2) & 1) * NT0;
    pc[0] = ld16<F>(kh ? rv : rk, ((t0 + 4 * j) & 63) * 1024 + lane * 16);
    pd[0] = (kh ? L_V : L_K) + t0 * 1024 + lane * 16;
  }
  const bf16x8 qf = __builtin_bit_cast(bf16x8, ld16<F>(rq, (j * 8 + qt) * 1024 + lane * 16));
  {
    const unsigned v = (lane >> 2) == 0 || (lane >> 2) == 4 ? 0x3F803F80u : 0u;
    *(LAS unsigned*)(lds + L_ONE + wid * 256 + lane * 4) = v;
    if (wid == 0 && lane < 2) *(LAS unsigned*)(lds + L_MISC + lane * 4) = 0u;
  }
  unsigned kaddr = L_K + kh * NT0 * 1024 + lane * 16, vaddr, vstep;
  if (r32 < 16) { vaddr = L_V + kh * NT0 * 1024 + hh * 256 + r32 * 16; vstep = 2048; }
  else { vaddr = L_ONE + hh * 256 + ((r32 >= 20 && r32 < 28) ? 64 : 0) + ((r32 == 16 || r32 == 20) ? 0 : 16); vstep = 0; }
  const unsigned kaddr0 = kaddr, vaddr0 = vaddr;
  f32x16 o = {};
  const f32x16 zero = {};
  STAMP(9);
  *(LAS u32x4*)(lds + pd[0]) = pc[0];
  SB();
#pragma unroll
  for (int n = 1; n < 8; ++n) {
    const int i = (n - 1) * 16 + wid, ti = i >> 1;
    const int t = ti < NT0 - 4 ? 4 + ti : 8 + ti;
    pc[n] = ld16<F>((wid & 1) ? rv : rk, ((t + 4 * j) & 63) * 1024 + lane * 16);
    pd[n] = ((wid & 1) ? L_V : L_K) + t * 1024 + lane * 16;
  }
  WAITL0();
  BAR();
  STAMP(10);
  SETP(3); attn_tile(lds, kaddr, vaddr, 0, qf, o, zero);
  *(LAS u32x4*)(lds + pd[1]) = pc[1]; *(LAS u32x4*)(lds + pd[2]) = pc[2];
  SETP(2); attn_tile(lds, kaddr, vaddr, 1024, qf, o, zero);
  *(LAS u32x4*)(lds + pd[3]) = pc[3]; *(LAS u32x4*)(lds + pd[4]) = pc[4];
  kaddr += 2048; vaddr += vstep;
  SETP(1); attn_tile(lds, kaddr, vaddr, 0, qf, o, zero);
  *(LAS u32x4*)(lds + pd[5]) = pc[5]; *(LAS u32x4*)(lds + pd[6]) = pc[6];
  SETP(0); attn_tile(lds, kaddr, vaddr, 1024, qf, o, zero);
  *(LAS u32x4*)(lds + pd[7]) = pc[7];
  kaddr += 2048; vaddr += vstep;
  if (F) WAITV(0);
  WAITL0();
  STAMP(11);
  BAR();
  if (F && wid == 0 && lane == 0) {
    const int b = blockIdx.x, xcd = b & 7, idx = b >> 3;
    if ((idx >> 3) & 1) set_flag(p.ws, WS_FB2 + (((xcd & 1) * 8 + (idx & 7)) * 8 + (xcd >> 1) * 2 + (idx >> 4)) * 8, tag);
  }
  if (F && kh == 1) {
    const char* a0 = p.woA + (size_t)(w3 * 64 + lane) * 1024 + cb * 128;
    const char* a1 = p.woB + (size_t)(w3 * 64 + lane) * 1024 + cb * 128;
    asm volatile("global_load_dword %0, %2, off\n\tglobal_load_dword %1, %3, off" : "=&v"(warm0), "=&v"(warm1) : "v"(a0), "v"(a1) : "memory");
  }
  SETP(3);
  const int nit = kh ? (60 - NT0) / 2 : (NT0 - 4) / 2;
#pragma unroll 1
  for (int it = nit; it > 2; --it) {
    if (it == 6) SETP(2);
    if (it == 3) SETP(1);
    attn_tile(lds, kaddr, vaddr, 0, qf, o, zero);
    attn_tile(lds, kaddr, vaddr, 1024, qf, o, zero);
    kaddr += 2048; vaddr += vstep;
  }
  if (F && kh == 1) load_ob(0, 8);
  SB();
  attn_tile(lds, kaddr, vaddr, 0, qf, o, zero);
  SB();
  attn_tile(lds, kaddr, vaddr, 1024, qf, o, zero);
  SB();
  kaddr += 2048; vaddr += vstep;
  SETP(0);
  attn_tile(lds, kaddr, vaddr, 0, qf, o, zero);
  SB();
  attn_tile(lds, kaddr, vaddr, 1024, qf, o, zero);
  SB();
  STAMP(13);
  auto rO = mkrs(p.ws + WS_OA, FRAG_BYTES);
  const unsigned oo = ((j * 8 + qt) * 32 + h) * 1024 + lane * 16;
  if (kh == 1) {
#pragma unroll
    for (int i = 0; i < 9; ++i) red[(qt * 9 + i) * 64 + lane] = o[i];
  }
  WAITL0();
  BAR();
  if (kh == 0) {
    bool ok = true;
#pragma unroll
    for (int i = 0; i < 9; ++i) { o[i] += red[(qt * 9 + i) * 64 + lane]; ok = ok && (fabsf(o[i]) <= 3.0e38f); }
    ok = ok && (o[8] >= 8.67e-19f);
    if (!__all(ok)) { if (lane == 0) *(LAS unsigned*)(lds + L_MISC + 4) = 1u; }
  }
  WAITL0();
  BAR();
  if (*(LAS volatile unsigned*)(lds + L_MISC + 4)) {
    const int nt = kh ? 64 - NT0 : NT0;
    float mx = -3.0e38f;
    for (int t = 0; t < nt; ++t) {
      f32x16 s = MFMA(lds_b128(lds, kaddr0 + t * 1024), qf, zero);
#pragma unroll
      for (int i = 0; i < 16; ++i) mx = fmaxf(mx, s[i]);
    }
    { auto rr = __builtin_amdgcn_permlane32_swap(__float_as_uint(mx), __float_as_uint(mx), false, false);
      mx = fmaxf(__uint_as_float(rr[0]), __uint_as_float(rr[1])); }
    red[wid * 64 + lane] = mx;
    WAITL0(); BAR();
    mx = fmaxf(mx, red[(wid ^ 8) * 64 + lane]);
    WAITL0(); BAR();
    f32x16 c;
#pragma unroll
    for (int i = 0; i < 16; ++i) { c[i] = -mx; o[i] = 0.f; }
    unsigned va = vaddr0;
    for (int t = 0; t < nt; ++t) { attn_tile(lds, kaddr0 + t * 1024, va, 0, qf, o, c); va += vstep >> 1; }
    if (kh == 1) {
#pragma unroll
      for (int i = 0; i < 9; ++i) red[(qt * 9 + i) * 64 + lane] = o[i];
    }
    WAITL0(); BAR();
    if (kh == 0) {
#pragma unroll
      for (int i = 0; i < 9; ++i) o[i] += red[(qt * 9 + i) * 64 + lane];
    }
    WAITL0(); BAR();
    if (F && kh == 1) load_ob(0, 8);
  }
  if (kh == 0) {
    st16<F>(rO, oo, pack8(o, 0, __builtin_amdgcn_rcpf(o[8])));
    if (F) {
      WAITV(0);
      unsigned old = 0;
      if (lane == 0) old = __hip_atomic_fetch_add((LAS unsigned*)(lds + L_MISC), 1u, __ATOMIC_RELAXED, __HIP_MEMORY_SCOPE_WORKGROUP);
      if (lane == 0 && old == 7) set_flag(p.ws, WS_FA2 + (j * 32 + h) * 8, tag);
    }
  }
  STAMP(14);
  }
  if (PH == 2) return;
  f32x16 acc = {}, accA = {};
  float gate = 0.f;
  if (kh == 1) {
    const int row = lane >> 3, pos = (lane & 7) ^ (((row >> 1) & 1) << 2);
    const size_t so = (size_t)(w3 * 8 + row) * 1024 + cb * 128 + pos * 16;
#pragma unroll
    for (int n = 0; n < 8; ++n) glds16(p.woB + so + n * 65536, ldsb + L_WB + (n * 8 + w3) * 1024);
    if (!F) load_ob(0, 8);
    load_ob(8, 16);
#pragma unroll
    for (int n = 0; n < 8; ++n) glds16(p.woA + so + n * 65536, ldsb + L_WA + (n * 8 + w3) * 1024);
    if (F) WAITV(16); else WAITV(24);
    asm volatile("" :: "v"(warm0), "v"(warm1));
    STAMP(17);
  }
  BAR();
  if (kh == 1) {
    STAMP(18);
#pragma unroll
    for (int i = 0; i < 16; ++i) acc = MFMA(lds_tr2(lds, L_WB + wa + i * 2048, 1024), __builtin_bit_cast(bf16x8, of[i]), acc);
    STAMP(19);
    if (F) poll_flags(p.ws, WS_FA2 + (j3 * 32 + kq * 16) * 8, 16, lane, tag);
    gate = __hip_atomic_load((GAS float*)(p.ws + WS_G) + T * 32 + r32, RLX_AGENT);
    {
      auto r = mkrs(p.ws + WS_OA, FRAG_BYTES);
#pragma unroll
      for (int i = 0; i < 16; ++i) of[i] = ld16<F>(r, (T * 32 + kq * 16 + i) * 1024 + lane * 16);
    }
    WAITV(16);
    STAMP(20);
  }
  BAR();
  LAS float* red3 = (LAS float*)(lds + L_RED3) + (wid & 3) * 1024;
  if (kh == 1) {
#pragma unroll
    for (int i = 0; i < 16; ++i) accA = MFMA(lds_tr2(lds, L_WA + wa + i * 2048, 1024), __builtin_bit_cast(bf16x8, of[i]), accA);
#pragma unroll
    for (int i = 0; i < 16; ++i) acc[i] = gate * accA[i] + (1.f - gate) * acc[i];
    STAMP(21);
    if (kq == 1) {
#pragma unroll
      for (int i = 0; i < 16; ++i) red3[i * 64 + lane] = acc[i];
    }
    WAITL0();
  }
  BAR();
  if (kh == 1 && kq == 0) {
#pragma unroll
    for (int i = 0; i < 16; ++i) acc[i] += red3[i * 64 + lane];
    unsigned w[8];
#pragma unroll
    for (int i = 0; i < 8; ++i) w[i] = cvtpk(acc[2 * i], acc[2 * i + 1]);
    char* dst = p.out + (size_t)(T * 32 + (lane & 31)) * 1024 + cb * 128 + ct * 64 + hh * 16;
#pragma unroll
    for (int g = 0; g < 4; g += 2) {
      auto a = __builtin_amdgcn_permlane32_swap(w[2 * g], w[2 * g + 2], false, false);
      auto b = __builtin_amdgcn_permlane32_swap(w[2 * g + 1], w[2 * g + 3], false, false);
      *(GAS u32x4*)(dst + g * 16) = u32x4{a[0], b[0], a[1], b[1]};
    }
  }
}

template <int PH>
__global__ __launch_bounds__(1024) void gated_mix_kernel(Params p) {
  extern __shared__ __attribute__((aligned(16))) char lds[];
  const int wid = __builtin_amdgcn_readfirstlane(threadIdx.x >> 6);
  const int lane = __builtin_amdgcn_mbcnt_hi(~0u, __builtin_amdgcn_mbcnt_lo(~0u, 0u));
  const int b = blockIdx.x, xcd = b & 7, idx = b >> 3;
  u64 tag = 0;
  if (PH == 0) tag = __hip_atomic_load((GAS u64*)(p.ws + WS_NONCE), RLX_AGENT) + 1;
  STAMP(0 + (PH >= 2 ? (PH - 1) * 8 : 0));
  if (PH == 0 || PH == 1) phase1<PH == 0>(p, lds, wid, lane, (idx >> 3) & 1, (xcd >> 1) * 2 + (idx >> 4), (xcd & 1) * 8 + (idx & 7), ((idx & 7) + 2 * (idx >> 3)) & 7, xcd * 4 + (idx >> 3), tag);
  if (PH == 0 || PH == 1) STAMP(4);
  tag = ((u64)__builtin_amdgcn_readfirstlane((unsigned)(tag >> 32)) << 32) | (unsigned)__builtin_amdgcn_readfirstlane((unsigned)tag);
  if (PH != 1) phase23<PH == 0>(p, lds, wid, lane, xcd * 4 + (idx >> 3), idx & 7, xcd * 4 + (idx >> 3), idx & 7, tag, PH);
  STAMP(23);
  if (b == DIAG_BLK && wid == DIAG_W) {
    const u64 ta = *(LAS volatile u64*)(lds + 163808), tb_ = *(LAS volatile u64*)(lds + 163816);
    const u64 t0 = __builtin_amdgcn_s_memrealtime();
    while (__builtin_amdgcn_s_memrealtime() - t0 < (tb_ - ta) * DIAG_K) __builtin_amdgcn_s_sleep(1);
  }
  if (PH == 0 && b == 0 && wid == 0 && lane == 0) set_flag(p.ws, WS_NONCE, tag);
}

extern "C" void kernel_launch(void* const* d_in, const int* in_sizes, int n_in, void* d_out, int out_size, void* d_ws, size_t ws_size,
                              hipStream_t stream) {
  Params p;
  p.x = (const char*)d_in[0]; p.wA = (const char*)d_in[1]; p.woA = (const char*)d_in[2];
  p.wB = (const char*)d_in[4]; p.woB = (const char*)d_in[5]; p.gw = (const char*)d_in[6];
  p.out = (char*)d_out; p.ws = (char*)d_ws;
  (void)hipFuncSetAttribute((const void*)gated_mix_kernel<0>, hipFuncAttributeMaxDynamicSharedMemorySize, LDS_TOTAL);
  gated_mix_kernel<0><<<256, 1024, LDS_TOTAL, stream>>>(p);
}
```

```cpp
#define GAPK 0
#define UNR_C 8
#define UNR_L 8
#include <hip/hip_runtime.h>
#include <hip/hip_bf16.h>
#include <stdint.h>
#include <type_traits>

#define FUSE 1

typedef unsigned long long u64;
using f32x16 = __attribute__((ext_vector_type(16))) float;
using bf16x8 = __attribute__((ext_vector_type(8))) short;
using s16x4 = __attribute__((ext_vector_type(4))) short;
using u32x4 = __attribute__((ext_vector_type(4))) unsigned;
typedef __bf16 bf2_t __attribute__((ext_vector_type(2)));
typedef float f2_t __attribute__((ext_vector_type(2)));
#define GAS __attribute__((address_space(1)))
#define LAS __attribute__((address_space(3)))
#define MFMA(a, b, c) __builtin_amdgcn_mfma_f32_32x32x16_bf16((a), (b), (c), 0, 0, 0)
#define RLX_AGENT __ATOMIC_RELAXED, __HIP_MEMORY_SCOPE_AGENT
#define BAR() asm volatile("s_barrier" ::: "memory")
#define WAITV(n) asm volatile("s_waitcnt vmcnt(" #n ")" ::: "memory")
#define WAITL0() asm volatile("s_waitcnt lgkmcnt(0)" ::: "memory")
#define SETP(x) __builtin_amdgcn_s_setprio(x)
#define SB() __builtin_amdgcn_sched_barrier(0)
#define OPAQUE(x) asm volatile("" : "+v"(x))

constexpr size_t MB = 1u << 20;
constexpr size_t WS_NONCE = 0, WS_TMO = 64;
constexpr size_t WS_FA1 = 4096;
constexpr size_t WS_FB2 = 16384;
constexpr size_t WS_FA2 = 20480;
constexpr size_t WS_G = 40960;
constexpr size_t WS_QA = 1 * MB, WS_KA = 3 * MB, WS_VA = 5 * MB, WS_OA = 7 * MB, WS_OB = 9 * MB;
constexpr unsigned FRAG_BYTES = 2u << 20;
constexpr int LDS_TOTAL = 160 * 1024;
constexpr int L_XR = 0, XSLOT = 160 * 128, L_WR = 3 * XSLOT, WSLOT = 3 * 64 * 128, L_GW = L_WR + 3 * WSLOT;
constexpr int L_BK = 0, L_BV = 20480, L_BQ = 40960;
constexpr int L_K = 0, L_V = 65536, L_ONE = 131072, L_RED = L_ONE + 4096, L_MISC = L_RED + 8 * 9 * 256;
constexpr int L_WB = 0, L_WA = 65536, L_RED3 = 131072;
constexpr int L_EX = 0;
static_assert(L_GW + 1024 <= LDS_TOTAL && L_MISC + 256 <= LDS_TOTAL, "lds");
constexpr float QSCALE = 0.25f * 1.4426950408889634f;
constexpr unsigned SPIN_CAP = 1u << 20;

struct Params {
  const char* x; const char* wA; const char* woA; const char* wB; const char* woB; const char* gw; char* out; char* ws;
};

__device__ __forceinline__ unsigned cvtpk(float lo, float hi) {
  f2_t v = {lo, hi};
  return __builtin_bit_cast(unsigned, __builtin_convertvector(v, bf2_t));
}
__device__ __forceinline__ u32x4 pack8(const f32x16& a, int base, float s) {
  u32x4 r;
#pragma unroll
  for (int i = 0; i < 4; ++i) r[i] = cvtpk(a[base + 2 * i] * s, a[base + 2 * i + 1] * s);
  return r;
}
__device__ __forceinline__ u32x4 pack8n(const f32x16& a, int base) {
  u32x4 r;
#pragma unroll
  for (int i = 0; i < 4; ++i) r[i] = cvtpk(a[base + 2 * i], a[base + 2 * i + 1]);
  return r;
}
template <bool F> __device__ __forceinline__ void st16(__amdgpu_buffer_rsrc_t r, unsigned off, u32x4 v) {
  __builtin_amdgcn_raw_buffer_store_b128(v, r, off, 0, F ? 16 : 0);
}
template <bool F> __device__ __forceinline__ u32x4 ld16(__amdgpu_buffer_rsrc_t r, unsigned off) {
  return __builtin_amdgcn_raw_buffer_load_b128(r, off, 0, F ? 16 : 0);
}
__device__ __forceinline__ __amdgpu_buffer_rsrc_t mkrs(const void* p, unsigned bytes) {
  return __builtin_amdgcn_make_buffer_rsrc((void*)p, 0, bytes, 0x00020000);
}
template <class T> __device__ __forceinline__ LAS T* lp(unsigned a) { return (LAS T*)(uintptr_t)a; }
__device__ __forceinline__ bf16x8 lds_b128(unsigned a) { return *lp<const bf16x8>(a); }
__device__ __forceinline__ bf16x8 lds_tr2(unsigned a, unsigned second) {
  s16x4 lo = __builtin_amdgcn_ds_read_tr16_b64_v4i16(lp<s16x4>(a));
  s16x4 hi = __builtin_amdgcn_ds_read_tr16_b64_v4i16(lp<s16x4>(a + second));
  return __builtin_shufflevector(lo, hi, 0, 1, 2, 3, 4, 5, 6, 7);
}
__device__ __forceinline__ void glds16s(const void* sbase, unsigned voff, unsigned dst) {
  unsigned sv;
  asm volatile("s_mov_b32 %0, m0\n\ts_mov_b32 m0, %3\n\ts_nop 0\n\tglobal_load_lds_dwordx4 %1, %2\n\ts_mov_b32 m0, %0" : "=&s"(sv) : "v"(voff), "s"(sbase), "s"(dst) : "memory");
}
template <int N> __device__ __forceinline__ void waitv() { asm volatile("s_waitcnt vmcnt(%0)" :: "n"(N) : "memory"); }

#define STAMP(i)

__device__ __forceinline__ void poll_flag_at(char* ws, size_t off, int lane, u64 tag) {
  GAS u64* f = (GAS u64*)(ws + off);
  unsigned spins = 0;
  while (true) {
    u64 v = __hip_atomic_load(f, RLX_AGENT);
    if (__all(v == tag)) break;
    __builtin_amdgcn_s_sleep(1);
    if (++spins > SPIN_CAP) { if (lane == 0) __hip_atomic_store((GAS unsigned*)(ws + WS_TMO), 1u, RLX_AGENT); break; }
  }
}
__device__ __forceinline__ void poll_flags(char* ws, size_t off, int n, int lane, u64 tag) { poll_flag_at(ws, off + (lane < n ? lane : 0) * 8, lane, tag); }
__device__ __forceinline__ void set_flag(char* ws, size_t off, u64 tag) { __hip_atomic_store((GAS u64*)(ws + off), tag, RLX_AGENT); }

struct P1Loader {
  const char* xs; const char* wsrc; unsigned xo, wo; int lw, rot, halo_fix;
  __device__ __forceinline__ void init(const Params& p, int lw_, int lane, int br, int hq, int tb, int rot_) {
    lw = lw_; rot = rot_;
    const int qs = (lane & 7) ^ ((lane >> 4) & 3) ^ (4 * (lw & 1));
    xs = p.x + (ptrdiff_t)(tb * 128 - 32) * 1024;
    xo = (lane >> 3) * 1024 + qs * 16;
    halo_fix = tb == 0 ? 32 * 1024 : 0;
    wsrc = (br ? p.wB : p.wA) + (size_t)(8 * lw) * 3072 + hq * 128;
    wo = (lane >> 3) * 3072 + (((lane & 7) ^ (((lane >> 4) & 1) << 2)) * 16);
  }
  __device__ __forceinline__ void unit(int u, int slot, bool halo) {
    const int ku = (u + rot) & 7;
    const unsigned xd = L_XR + slot * XSLOT, wd = L_WR + slot * WSLOT + lw * 1024;
    const char* xu = xs + ku * 128;
    const char* wu = wsrc + (size_t)ku * 64 * 3072;
    glds16s(xu + (4 + lw) * 8192, xo, xd + (4 + lw) * 1024);
    glds16s(wu, wo, wd);
    glds16s(xu + (12 + lw) * 8192, xo, xd + (12 + lw) * 1024);
    glds16s(wu + 1024, wo, wd + 8192);
    glds16s(wu + 2048, wo, wd + 16384);
    if (halo) glds16s(xu + lw * 8192 + halo_fix, xo, xd + lw * 1024);
  }
};

__device__ __forceinline__ void local_unit(const Params& p, int hl, int tt, int h, int T, int lane) {
  const int r32 = lane & 31, hh = lane >> 5;
  const bf16x8 q = lds_b128(L_BQ + (hl * 4 + tt) * 1024 + lane * 16);
  const bf16x8 kp = lds_b128(L_BK + (hl * 5 + tt) * 1024 + lane * 16), kc = lds_b128(L_BK + (hl * 5 + tt + 1) * 1024 + lane * 16);
  const unsigned vo = L_BV + (hl * 5 + tt) * 1024 + hh * 256 + (r32 & 15) * 16;
  u32x4 vp = *lp<const u32x4>(vo + 512), v0 = *lp<const u32x4>(vo + 1024), v1 = *lp<const u32x4>(vo + 1536);
  if (r32 >= 16) {
    const unsigned o = (r32 == 16 || r32 == 20) ? 0x3F803F80u : 0u;
    v0 = u32x4{o, o, o, o}; v1 = v0; vp = v0;
  }
  u64 m = 0x5555ull << (18 + r32);
  if (T == 0) m &= 0xFFFFFFFF00000000ull;
  m = ~(m >> (4 * hh));
  const int mlo = (int)(unsigned)m, mhi = (int)(unsigned)(m >> 32);
  const int NEG = 0xf149f2ca;
  f32x16 cc, cp;
#pragma unroll
  for (int i = 0; i < 16; ++i) {
    const int b = (i & 3) + 8 * (i >> 2);
    cc[i] = __int_as_float(__builtin_amdgcn_sbfe(mhi, b, 1) & NEG);
    cp[i] = i >= 8 ? __int_as_float(__builtin_amdgcn_sbfe(mlo, b, 1) & NEG) : 0.f;
  }
  f32x16 sc = MFMA(kc, q, cc);
  f32x16 sp = MFMA(kp, q, cp);
  float mx = fmaxf(sc[0], sc[1]);
#pragma unroll
  for (int i = 2; i < 16; i += 2) mx = fmaxf(fmaxf(mx, sc[i]), sc[i + 1]);
#pragma unroll
  for (int i = 8; i < 16; i += 2) mx = fmaxf(fmaxf(mx, sp[i]), sp[i + 1]);
  {
    auto rr = __builtin_amdgcn_permlane32_swap(__float_as_uint(mx), __float_as_uint(mx), false, false);
    mx = fmaxf(__uint_as_float(rr[0]), __uint_as_float(rr[1]));
  }
  u32x4 p0, p1, pp;
#pragma unroll
  for (int i = 0; i < 4; ++i) {
    p0[i] = cvtpk(__builtin_amdgcn_exp2f(sc[2 * i] - mx), __builtin_amdgcn_exp2f(sc[2 * i + 1] - mx));
    p1[i] = cvtpk(__builtin_amdgcn_exp2f(sc[8 + 2 * i] - mx), __builtin_amdgcn_exp2f(sc[8 + 2 * i + 1] - mx));
    pp[i] = cvtpk(__builtin_amdgcn_exp2f(sp[8 + 2 * i] - mx), __builtin_amdgcn_exp2f(sp[8 + 2 * i + 1] - mx));
  }
  f32x16 o = {};
  o = MFMA(__builtin_bit_cast(bf16x8, v0), __builtin_bit_cast(bf16x8, p0), o);
  o = MFMA(__builtin_bit_cast(bf16x8, v1), __builtin_bit_cast(bf16x8, p1), o);
  o = MFMA(__builtin_bit_cast(bf16x8, vp), __builtin_bit_cast(bf16x8, pp), o);
  __builtin_amdgcn_raw_buffer_store_b128(pack8(o, 0, __builtin_amdgcn_rcpf(o[8])), mkrs(p.ws + WS_OB, FRAG_BYTES), (T * 32 + h) * 1024 + lane * 16, 0, 16);
}

template <bool F>
__device__ __forceinline__ void phase1(const Params& p, int wid, int lane, int br, int hq, int tb, int rot, int h2, u64 tagv) {
  const int r32 = lane & 31, hh = lane >> 5, q4 = (lane & 15) >> 2;
  const bool main = wid < 8, halo = br && wid >= 8 && wid < 12;
  const int tt = main ? (wid & 3) + 1 : 0, half = main ? wid >> 2 : (wid >> 1) & 1;
  const int row = 32 * tt + r32, f = (row >> 1) & 7;
  const unsigned xa0 = L_XR + row * 128 + (((hh ^ (f & 1)) | (f & 6)) * 16);
  const unsigned wa0 = L_WR + (8 * hh + q4) * 128 + ((half ^ ((q4 >> 1) & 1)) * 64) + ((lane >> 4) & 1) * 32 + (lane & 3) * 8;
  f32x16 aq = {}, ak = {}, av = {};
  if (main) {
    BAR();
    STAMP(1);
    unsigned xa[4], wa = wa0;
#pragma unroll
    for (int m = 0; m < 4; ++m) xa[m] = xa0 ^ (m << 5);
    OPAQUE(wa);
    bf16x8 xf[4], wq[4], wk[4], wv[4];
#pragma unroll
    for (int m = 0; m < 2; ++m) {
      SB(); xf[m] = lds_b128(xa[m]); wq[m] = lds_tr2(wa + m * 2048, 512);
      SB(); wk[m] = lds_tr2(wa + m * 2048 + 8192, 512);
      SB(); wv[m] = lds_tr2(wa + m * 2048 + 16384, 512);
    }
    SB();
    int slot = 0;
#pragma unroll UNR_C
    for (int u = 0; u < 8; ++u) {
      const int nslot = slot == 2 ? 0 : slot + 1;
      const int dx = (nslot - slot) * XSLOT, dw = (nslot - slot) * WSLOT;
#pragma unroll
      for (int m = 0; m < 4; ++m) {
        if (m == 2) {
          wa += dw;
#pragma unroll
          for (int i = 0; i < 4; ++i) xa[i] += dx;
        }
        const int mn = (m + 2) & 3;
        SB();
        aq = MFMA(wq[m], xf[m], aq);
        SB();
        xf[mn] = lds_b128(xa[mn]); wq[mn] = lds_tr2(wa + mn * 2048, 512);
        SB();
        ak = MFMA(wk[m], xf[m], ak);
        SB();
        wk[mn] = lds_tr2(wa + mn * 2048 + 8192, 512);
        SB();
        av = MFMA(xf[m], wv[m], av);
        SB();
        wv[mn] = lds_tr2(wa + mn * 2048 + 16384, 512);
        if (m & 1) BAR();
      }
      slot = nslot;
    }
  } else if (halo) {
    SETP(3);
    P1Loader L; L.init(p, wid - 8, lane, br, hq, tb, rot);
    L.unit(0, 0, true); L.unit(1, 1, true);
    waitv<6>();
    BAR();
    auto halo_loop = [&](auto ISV) {
      constexpr bool isv = decltype(ISV)::value;
      unsigned xa[4], wa = wa0 + (isv ? 16384 : 8192);
#pragma unroll
      for (int m = 0; m < 4; ++m) xa[m] = xa0 ^ (m << 5);
      OPAQUE(wa);
      bf16x8 xf[2], wf[2];
      f32x16 a = {};
      xf[0] = lds_b128(xa[0]); wf[0] = lds_tr2(wa, 512);
      int slot = 0;
#pragma unroll UNR_L
      for (int u = 0; u < 8; ++u) {
        const int nslot = slot == 2 ? 0 : slot + 1;
        const int dx = (nslot - slot) * XSLOT, dw = (nslot - slot) * WSLOT;
        if (u + 2 < 8) L.unit(u + 2, nslot == 2 ? 0 : nslot + 1, true);
#pragma unroll
        for (int m = 0; m < 4; ++m) {
          const int c = m & 1, n = c ^ 1;
          if (m == 3) {
            wa += dw;
#pragma unroll
            for (int i = 0; i < 4; ++i) xa[i] += dx;
          }
          const int mn = (m + 1) & 3;
          SB();
          if (isv) a = MFMA(xf[c], wf[c], a); else a = MFMA(wf[c], xf[c], a);
          SB();
          xf[n] = lds_b128(xa[mn]); wf[n] = lds_tr2(wa + mn * 2048, 512);
          if (m == 1) { if (u + 2 < 8) waitv<6>(); else waitv<0>(); }
          if (m & 1) BAR();
        }
        slot = nslot;
      }
      return a;
    };
    if (wid & 1) av = halo_loop(std::true_type{}); else ak = halo_loop(std::false_type{});
  } else {
    SETP(3);
    P1Loader L; L.init(p, wid - 8, lane, br, hq, tb, rot);
    const bool gatew = !br && wid == 8;
    if (gatew) glds16s(p.gw, lane * 16, L_GW);
    L.unit(0, 0, false); L.unit(1, 1, false);
    waitv<5>();
    BAR();
    float ga = 0.f;
    const int grow = 32 + 16 * hq + (lane >> 2), gf = (grow >> 1) & 7;
    int slot = 0;
#pragma unroll UNR_L
    for (int u = 0; u < 8; ++u) {
      const int nslot = slot == 2 ? 0 : slot + 1;
      if (u + 2 < 8) L.unit(u + 2, nslot == 2 ? 0 : nslot + 1, false);
      if (gatew) {
#pragma unroll
        for (int e = 0; e < 2; ++e) {
          const int q = 2 * (lane & 3) + e;
          const u32x4 xv = *lp<const u32x4>(L_XR + slot * XSLOT + grow * 128 + ((q ^ gf) * 16));
          const u32x4 gv = *lp<const u32x4>(L_GW + ((u + rot) & 7) * 128 + q * 16);
#pragma unroll
          for (int d = 0; d < 4; ++d) {
            const unsigned xa = xv[d], gb = gv[d];
            ga = __builtin_amdgcn_fdot2_f32_bf16(__builtin_bit_cast(bf2_t, xa), __builtin_bit_cast(bf2_t, gb), ga, false);
          }
        }
      }
      if (u + 2 < 8) waitv<5>(); else waitv<0>();
      BAR();
      BAR();
      slot = nslot;
    }
    if (gatew) {
      ga += __shfl_xor(ga, 1); ga += __shfl_xor(ga, 2);
      const float g = 1.f / (1.f + __expf(-ga));
      if ((lane & 3) == 0) __hip_atomic_store((GAS float*)(p.ws + WS_G) + tb * 128 + hq * 16 + (lane >> 2), g, RLX_AGENT);
    }
  }
  SETP(0);
  STAMP(2);
  const int h0 = 2 * half;
  GAS u64* fa1 = (GAS u64*)(p.ws + WS_FA1) + (h2 >> 1) * 64 + lane;
  if (!br) {
    if (main) {
      const int T = tb * 4 + tt - 1, hg = 4 * hq + h0;
      auto rq = mkrs(p.ws + WS_QA, FRAG_BYTES), rk = mkrs(p.ws + WS_KA, FRAG_BYTES), rv = mkrs(p.ws + WS_VA, FRAG_BYTES);
      const unsigned o0 = (hg * 64 + T) * 1024 + lane * 16;
      st16<F>(rk, o0, pack8n(ak, 0));
      st16<F>(rk, o0 + 65536, pack8n(ak, 8));
      const unsigned ov = ((hg + ((lane >> 4) & 1)) * 64 + T) * 1024 + hh * 256 + (lane & 15) * 16;
      st16<F>(rv, ov, pack8n(av, 0));
      st16<F>(rv, ov + 512, pack8n(av, 8));
      st16<F>(rq, o0, pack8(aq, 0, QSCALE));
      st16<F>(rq, o0 + 65536, pack8(aq, 8, QSCALE));
      STAMP(3);
      if (F) {
        WAITV(0);
        STAMP(5);
        if (wid != 0 && lane == 0) set_flag(p.ws, WS_FA1 + ((hg >> 1) * 64 + T) * 8, tagv);
      }
    }
    if (F) {
      if (wid == 8) WAITV(0);
      BAR();
      STAMP(6);
      if (wid == 0 && lane == 0) set_flag(p.ws, WS_FA1 + ((2 * hq) * 64 + tb * 4) * 8, tagv);
      if (wid == 15) poll_flag_at(p.ws, (char*)fa1 - p.ws, lane, tagv);
      STAMP(7);
      BAR();
    }
  } else {
    if (main || (halo && !(wid & 1))) {
      LAS u32x4* kd = lp<u32x4>(L_BK + (h0 * 5 + tt) * 1024 + lane * 16);
      kd[0] = pack8n(ak, 0); kd[5 * 64] = pack8n(ak, 8);
    }
    if (main || (halo && (wid & 1))) {
      LAS u32x4* vd = lp<u32x4>(L_BV + ((h0 + ((lane >> 4) & 1)) * 5 + tt) * 1024 + hh * 256 + (lane & 15) * 16);
      vd[0] = pack8n(av, 0); vd[32] = pack8n(av, 8);
    }
    if (main) {
      LAS u32x4* qd = lp<u32x4>(L_BQ + (h0 * 4 + tt - 1) * 1024 + lane * 16);
      qd[0] = pack8(aq, 0, QSCALE); qd[4 * 64] = pack8(aq, 8, QSCALE);
    }
    STAMP(3);
    u64 seen = 0;
    WAITL0();
    BAR();
    if (F && wid == 15) seen = __hip_atomic_load(fa1, RLX_AGENT);
    STAMP(5);
    local_unit(p, wid >> 2, wid & 3, 4 * hq + (wid >> 2), tb * 4 + (wid & 3), lane);
    STAMP(6);
    if (F) {
      if (wid == 15 && !__all(seen == tagv)) poll_flag_at(p.ws, (char*)fa1 - p.ws, lane, tagv);
      STAMP(7);
      WAITL0();
      BAR();
    }
  }
}

__device__ __forceinline__ void attn_tile(unsigned ka, unsigned va, int off, const bf16x8& qf, f32x16& o, const f32x16& c) {
  bf16x8 kf = lds_b128(ka + off);
  bf16x8 v0 = lds_b128(va + off);
  bf16x8 v1 = lds_b128(va + off + 512);
  f32x16 s = MFMA(kf, qf, c);
  u32x4 p0, p1;
#pragma unroll
  for (int i = 0; i < 4; ++i) p0[i] = cvtpk(__builtin_amdgcn_exp2f(s[2 * i]), __builtin_amdgcn_exp2f(s[2 * i + 1]));
#pragma unroll
  for (int i = 0; i < 4; ++i) p1[i] = cvtpk(__builtin_amdgcn_exp2f(s[8 + 2 * i]), __builtin_amdgcn_exp2f(s[8 + 2 * i + 1]));
  o = MFMA(v0, __builtin_bit_cast(bf16x8, p0), o);
  o = MFMA(v1, __builtin_bit_cast(bf16x8, p1), o);
}

template <bool F>
__device__ __forceinline__ void phase23(const Params& p, int wid, int lane, int h, int j, int tb3, int cb, u64 tag, int PH) {
  const int qt = wid & 7, kh = wid >> 3, r32 = lane & 31, hh = lane >> 5;
  LAS float* red = lp<float>(L_RED);
  const int tt = qt & 1, kq = qt >> 1;
  const int T = tb3 * 2 + tt;
  const int q4 = (lane & 15) >> 2;
  u32x4 of[8];
  unsigned warm0 = 0, warm1 = 0;
  auto load_ob = [&]() {
    if (F) poll_flags(p.ws, WS_FB2 + ((tb3 >> 1) * 8 + kq * 2) * 8, 2, lane, tag);
    auto r = mkrs(p.ws + WS_OB, FRAG_BYTES);
#pragma unroll
    for (int i = 0; i < 8; ++i) of[i] = ld16<F>(r, (T * 32 + kq * 8 + i) * 1024 + lane * 16);
  };
  if (PH != 3) {
    auto rq = mkrs(p.ws + WS_QA + (size_t)h * 65536, 65536), rk = mkrs(p.ws + WS_KA + (size_t)h * 65536, 65536), rv = mkrs(p.ws + WS_VA + (size_t)h * 65536, 65536);
    u32x4 pc[8];
    const int t0 = (wid & 3) + ((wid >> 2) & 1) * 32;
    pc[0] = ld16<F>(kh ? rv : rk, ((t0 + 4 * j) & 63) * 1024 + lane * 16);
    const bf16x8 qf = __builtin_bit_cast(bf16x8, ld16<F>(rq, (j * 8 + qt) * 1024 + lane * 16));
    {
      const unsigned v = (lane >> 2) == 0 || (lane >> 2) == 4 ? 0x3F803F80u : 0u;
      *lp<unsigned>(L_ONE + wid * 256 + lane * 4) = v;
      if (wid == 0 && lane == 0) *lp<unsigned>(L_MISC) = 0u;
    }
    unsigned kaddr = L_K + kh * 32768 + lane * 16, vaddr, vstep;
    if (r32 < 16) { vaddr = L_V + kh * 32768 + hh * 256 + r32 * 16; vstep = 2048; }
    else { vaddr = L_ONE + hh * 256 + ((r32 >= 20 && r32 < 28) ? 64 : 0) + ((r32 == 16 || r32 == 20) ? 0 : 16); vstep = 0; }
    const unsigned kaddr0 = kaddr, vaddr0 = vaddr;
    const unsigned pdb = ((wid & 1) ? L_V : L_K) + lane * 16;
    auto tile_of = [&](int n) { const int ti = ((n - 1) * 16 + wid) >> 1; return ti < 28 ? 4 + ti : 8 + ti; };
    auto load_rest = [&](int n0, int n1) {
#pragma unroll
      for (int n = 1; n < 8; ++n) if (n >= n0 && n < n1) pc[n] = ld16<F>((wid & 1) ? rv : rk, ((tile_of(n) + 4 * j) & 63) * 1024 + lane * 16);
    };
    auto put = [&](int n) { *lp<u32x4>(pdb + tile_of(n) * 1024) = pc[n]; };
    f32x16 o = {};
    const f32x16 zero = {};
    BAR();
    load_rest(1, 4);
    STAMP(9);
    *lp<u32x4>((kh ? L_V : L_K) + t0 * 1024 + lane * 16) = pc[0];
    WAITL0();
    BAR();
    STAMP(10);
    if (kh) load_rest(4, 8);
    SB();
    SETP(3); attn_tile(kaddr, vaddr, 0, qf, o, zero);
    SB();
    if (!kh) load_rest(4, 8);
    put(1); put(2);
    SB();
    SETP(2); attn_tile(kaddr, vaddr, 1024, qf, o, zero);
    put(3);
    kaddr += 2048; vaddr += vstep;
    SETP(1); attn_tile(kaddr, vaddr, 0, qf, o, zero);
    put(4); put(5);
    SETP(0); attn_tile(kaddr, vaddr, 1024, qf, o, zero);
    put(6); put(7);
    kaddr += 2048; vaddr += vstep;
    if (F) WAITV(0);
    WAITL0();
    STAMP(11);
    BAR();
    if (F && wid == 0 && lane == 0) {
      const int b = blockIdx.x, xcd = b & 7, idx = b >> 3;
      if ((idx >> 3) & 1) set_flag(p.ws, WS_FB2 + (((xcd & 1) * 8 + (idx & 7)) * 8 + (xcd >> 1) * 2 + (idx >> 4)) * 8, tag);
    }
    if (F && kh == 1) {
      const char* a0 = p.woA + (size_t)(qt * 64 + lane) * 1024 + cb * 128;
      const char* a1 = p.woB + (size_t)(qt * 64 + lane) * 1024 + cb * 128;
      asm volatile("global_load_dword %0, %2, off\n\tglobal_load_dword %1, %3, off" : "=&v"(warm0), "=&v"(warm1) : "v"(a0), "v"(a1) : "memory");
    }
    SETP(3);
#pragma unroll 1
    for (int it = 14; it > 2; --it) {
      if (it == 6) SETP(2);
      if (it == 3) SETP(1);
      attn_tile(kaddr, vaddr, 0, qf, o, zero);
      attn_tile(kaddr, vaddr, 1024, qf, o, zero);
      kaddr += 2048; vaddr += vstep;
    }
    if (F && kh == 1) load_ob();
#pragma unroll 1
    for (int it = 2; it > 0; --it) {
      if (it == 1) SETP(0);
      attn_tile(kaddr, vaddr, 0, qf, o, zero);
      attn_tile(kaddr, vaddr, 1024, qf, o, zero);
      kaddr += 2048; vaddr += vstep;
    }
    STAMP(13);
    auto rO = mkrs(p.ws + WS_OA, FRAG_BYTES);
    const unsigned oo = ((j * 8 + qt) * 32 + h) * 1024 + lane * 16;
    if (kh == 1) {
#pragma unroll
      for (int i = 0; i < 9; ++i) red[(qt * 9 + i) * 64 + lane] = o[i];
    }
    WAITL0();
    BAR();
    if (kh == 0) {
      bool ok = true;
#pragma unroll
      for (int i = 0; i < 9; ++i) { o[i] += red[(qt * 9 + i) * 64 + lane]; ok = ok && (fabsf(o[i]) <= 3.0e38f); }
      ok = ok && (o[8] >= 8.67e-19f);
      if (__all(ok)) st16<F>(rO, oo, pack8(o, 0, __builtin_amdgcn_rcpf(o[8])));
      else if (lane == 0) *lp<unsigned>(L_MISC) = 1u;
    }
    WAITL0();
    BAR();
    if (*lp<volatile unsigned>(L_MISC)) {
      float mx = -3.0e38f;
      for (int t = 0; t < 32; ++t) {
        f32x16 s = MFMA(lds_b128(kaddr0 + t * 1024), qf, zero);
#pragma unroll
        for (int i = 0; i < 16; ++i) mx = fmaxf(mx, s[i]);
      }
      { auto rr = __builtin_amdgcn_permlane32_swap(__float_as_uint(mx), __float_as_uint(mx), false, false);
        mx = fmaxf(__uint_as_float(rr[0]), __uint_as_float(rr[1])); }
      red[wid * 64 + lane] = mx;
      WAITL0(); BAR();
      mx = fmaxf(mx, red[(wid ^ 8) * 64 + lane]);
      WAITL0(); BAR();
      f32x16 c;
#pragma unroll
      for (int i = 0; i < 16; ++i) { c[i] = -mx; o[i] = 0.f; }
      unsigned va = vaddr0;
      for (int t = 0; t < 32; ++t) { attn_tile(kaddr0 + t * 1024, va, 0, qf, o, c); va += vstep >> 1; }
      if (kh == 1) {
#pragma unroll
        for (int i = 0; i < 9; ++i) red[(qt * 9 + i) * 64 + lane] = o[i];
      }
      WAITL0(); BAR();
      if (kh == 0) {
#pragma unroll
        for (int i = 0; i < 9; ++i) o[i] += red[(qt * 9 + i) * 64 + lane];
        st16<F>(rO, oo, pack8(o, 0, __builtin_amdgcn_rcpf(o[8])));
      }
      WAITL0(); BAR();
      if (F && kh == 1) load_ob();
    }
    if (F && kh == 0) {
      WAITV(0);
      if (lane == 0) set_flag(p.ws, WS_FA2 + ((j * 8 + qt) * 32 + h) * 8, tag);
    }
    STAMP(14);
  }
  if (PH == 2) return;
  const unsigned wa = (4 * hh + q4) * 128 + ((lane >> 4) & 1) * 32 + (lane & 3) * 8 + kq * 8 * 2048;
  const unsigned sw = ((q4 >> 1) & 1) * 64;
  f32x16 b0 = {}, b1 = {}, a0 = {}, a1 = {};
  float gate = 0.f;
  if (kh == 1) {
    const int row = lane >> 3;
    const unsigned so = row * 1024 + (((lane & 7) ^ (((row >> 1) & 1) << 2)) * 16);
#pragma unroll
    for (int n = 0; n < 8; ++n) glds16s(p.woB + (size_t)(n * 8 + qt) * 8192 + cb * 128, so, L_WB + (n * 8 + qt) * 1024);
    if (!F) load_ob();
#pragma unroll
    for (int n = 0; n < 8; ++n) glds16s(p.woA + (size_t)(n * 8 + qt) * 8192 + cb * 128, so, L_WA + (n * 8 + qt) * 1024);
    WAITV(8);
    asm volatile("" :: "v"(warm0), "v"(warm1));
    STAMP(17);
  }
  BAR();
  if (kh == 1) {
    STAMP(18);
#pragma unroll
    for (int i = 0; i < 8; ++i) {
      b0 = MFMA(lds_tr2(L_WB + wa + sw + i * 2048, 1024), __builtin_bit_cast(bf16x8, of[i]), b0);
      b1 = MFMA(lds_tr2(L_WB + wa + (sw ^ 64) + i * 2048, 1024), __builtin_bit_cast(bf16x8, of[i]), b1);
    }
    STAMP(19);
    if (F) poll_flags(p.ws, WS_FA2 + (T * 32 + kq * 8) * 8, 8, lane, tag);
    {
      auto r = mkrs(p.ws + WS_OA, FRAG_BYTES);
#pragma unroll
      for (int i = 0; i < 8; ++i) of[i] = ld16<F>(r, (T * 32 + kq * 8 + i) * 1024 + lane * 16);
    }
    WAITV(8);
    STAMP(20);
  }
  BAR();
  using f32x4 = __attribute__((ext_vector_type(4))) float;
  LAS f32x4* ex = lp<f32x4>(L_EX) + tt * (4 * 4 * 2 * 64) + lane;
  if (kh == 1) {
    gate = __hip_atomic_load((GAS float*)(p.ws + WS_G) + T * 32 + r32, RLX_AGENT);
#pragma unroll
    for (int i = 0; i < 8; ++i) {
      a0 = MFMA(lds_tr2(L_WA + wa + sw + i * 2048, 1024), __builtin_bit_cast(bf16x8, of[i]), a0);
      a1 = MFMA(lds_tr2(L_WA + wa + (sw ^ 64) + i * 2048, 1024), __builtin_bit_cast(bf16x8, of[i]), a1);
    }
#pragma unroll
    for (int i = 0; i < 16; ++i) { a0[i] = gate * a0[i] + (1.f - gate) * b0[i]; a1[i] = gate * a1[i] + (1.f - gate) * b1[i]; }
    STAMP(21);
#pragma unroll
    for (int ow = 0; ow < 4; ++ow) {
#pragma unroll
      for (int e = 0; e < 2; ++e) {
        const int i = 8 * (ow >> 1) + 4 * e;
        ex[((ow * 4 + kq) * 2 + e) * 64] = (ow & 1) ? f32x4{a1[i], a1[i + 1], a1[i + 2], a1[i + 3]} : f32x4{a0[i], a0[i + 1], a0[i + 2], a0[i + 3]};
      }
    }
    WAITL0();
  }
  BAR();
  if (kh == 1) {
    float s[8];
#pragma unroll
    for (int e = 0; e < 2; ++e) {
      const f32x4 v = (ex[((kq * 4 + 0) * 2 + e) * 64] + ex[((kq * 4 + 1) * 2 + e) * 64]) + (ex[((kq * 4 + 2) * 2 + e) * 64] + ex[((kq * 4 + 3) * 2 + e) * 64]);
      s[4 * e] = v[0]; s[4 * e + 1] = v[1]; s[4 * e + 2] = v[2]; s[4 * e + 3] = v[3];
    }
    const unsigned w0 = cvtpk(s[0], s[1]), w1 = cvtpk(s[2], s[3]), w2 = cvtpk(s[4], s[5]), w3 = cvtpk(s[6], s[7]);
    auto a = __builtin_amdgcn_permlane32_swap(w0, w2, false, false);
    auto b = __builtin_amdgcn_permlane32_swap(w1, w3, false, false);
    *(GAS u32x4*)(p.out + (size_t)(T * 32 + r32) * 1024 + cb * 128 + (kq & 1) * 64 + (kq >> 1) * 32 + hh * 16) = u32x4{a[0], b[0], a[1], b[1]};
  }
}

template <int PH>
__global__ __launch_bounds__(1024) void gated_mix_kernel(Params p) {
  const int wid = __builtin_amdgcn_readfirstlane(threadIdx.x >> 6);
  const int lane = __builtin_amdgcn_mbcnt_hi(~0u, __builtin_amdgcn_mbcnt_lo(~0u, 0u));
  const int b = blockIdx.x, xcd = b & 7, idx = b >> 3;
  u64 tag = 0;
  if (PH == 0) tag = __hip_atomic_load((GAS u64*)(p.ws + WS_NONCE), RLX_AGENT) + 1;
  STAMP(0 + (PH >= 2 ? (PH - 1) * 8 : 0));
  if (PH == 0 || PH == 1) phase1<PH == 0>(p, wid, lane, (idx >> 3) & 1, (xcd >> 1) * 2 + (idx >> 4), (xcd & 1) * 8 + (idx & 7), ((idx & 7) + 2 * (idx >> 3)) & 7, xcd * 4 + (idx >> 3), tag);
  if (PH == 0 || PH == 1) STAMP(4);
  tag = ((u64)__builtin_amdgcn_readfirstlane((unsigned)(tag >> 32)) << 32) | (unsigned)__builtin_amdgcn_readfirstlane((unsigned)tag);
  if (PH != 1) phase23<PH == 0>(p, wid, lane, xcd * 4 + (idx >> 3), idx & 7, xcd * 4 + (idx >> 3), idx & 7, tag, PH);
  STAMP(23);
  if (wid == 8 && lane == 0) __hip_atomic_store((GAS u64*)(p.ws + 57344) + b, (u64)__builtin_amdgcn_s_memrealtime(), RLX_AGENT);
  if (PH == 0 && b == 0 && wid == 8 && lane == 0) set_flag(p.ws, WS_NONCE, tag);
}

__global__ void tiny_kernel(char* ws) {
  const u64 t0 = __builtin_amdgcn_s_memrealtime();
  u64 m = 0;
  for (int i = 0; i < 4; ++i) { u64 v = __hip_atomic_load((GAS u64*)(ws + 57344) + threadIdx.x + 64 * i, RLX_AGENT); m = v > m ? v : m; }
  for (int d = 1; d < 64; d <<= 1) { u64 o = __shfl_xor(m, d); m = o > m ? o : m; }
  const u64 t1 = __builtin_amdgcn_s_memrealtime();
  while (__builtin_amdgcn_s_memrealtime() - t1 < (t0 - m) * GAPK) __builtin_amdgcn_s_sleep(1);
}
extern "C" void kernel_launch(void* const* d_in, const int* in_sizes, int n_in, void* d_out, int out_size, void* d_ws, size_t ws_size,
                              hipStream_t stream) {
  Params p;
  p.x = (const char*)d_in[0]; p.wA = (const char*)d_in[1]; p.woA = (const char*)d_in[2];
  p.wB = (const char*)d_in[4]; p.woB = (const char*)d_in[5]; p.gw = (const char*)d_in[6];
  p.out = (char*)d_out; p.ws = (char*)d_ws;
  (void)hipFuncSetAttribute((const void*)gated_mix_kernel<0>, hipFuncAttributeMaxDynamicSharedMemorySize, LDS_TOTAL);
  gated_mix_kernel<0><<<256, 1024, LDS_TOTAL, stream>>>(p);
  tiny_kernel<<<1, 64, 0, stream>>>((char*)d_ws);
}
```

```cpp
#include <hip/hip_runtime.h>
#include <hip/hip_bf16.h>
#include <stdint.h>
#include <type_traits>

#define UNR_C 8
#define UNR_L 8
#define FUSE 1

typedef unsigned long long u64;
using f32x16 = __attribute__((ext_vector_type(16))) float;
using bf16x8 = __attribute__((ext_vector_type(8))) short;
using s16x4 = __attribute__((ext_vector_type(4))) short;
using u32x4 = __attribute__((ext_vector_type(4))) unsigned;
typedef __bf16 bf2_t __attribute__((ext_vector_type(2)));
typedef float f2_t __attribute__((ext_vector_type(2)));
#define GAS __attribute__((address_space(1)))
#define LAS __attribute__((address_space(3)))
#define MFMA(a, b, c) __builtin_amdgcn_mfma_f32_32x32x16_bf16((a), (b), (c), 0, 0, 0)
#define RLX_AGENT __ATOMIC_RELAXED, __HIP_MEMORY_SCOPE_AGENT
#define BAR() asm volatile("s_barrier" ::: "memory")
#define WAITV(n) asm volatile("s_waitcnt vmcnt(" #n ")" ::: "memory")
#define WAITL0() asm volatile("s_waitcnt lgkmcnt(0)" ::: "memory")
#define SETP(x) __builtin_amdgcn_s_setprio(x)
#define SB() __builtin_amdgcn_sched_barrier(0)
#define OPAQUE(x) asm volatile("" : "+v"(x))

constexpr size_t MB = 1u << 20;
constexpr size_t WS_NONCE = 0, WS_TMO = 64;
constexpr size_t WS_FA1 = 4096;
constexpr size_t WS_FB2 = 16384;
constexpr size_t WS_FA2 = 20480;
constexpr size_t WS_G = 40960;
constexpr size_t WS_QA = 1 * MB, WS_KA = 3 * MB, WS_VA = 5 * MB, WS_OA = 7 * MB, WS_OB = 9 * MB;
constexpr unsigned FRAG_BYTES = 2u << 20;
constexpr int LDS_TOTAL = 160 * 1024;
constexpr int L_XR = 0, XSLOT = 160 * 128, L_WR = 3 * XSLOT, WSLOT = 3 * 64 * 128, L_GW = L_WR + 3 * WSLOT;
constexpr int L_BK = 0, L_BV = 20480, L_BQ = 40960;
constexpr int L_K = 0, L_V = 65536, L_ONE = 131072, L_RED = L_ONE + 4096, L_MISC = L_RED + 8 * 9 * 256;
__host__ __device__ constexpr int l_wb(int kq) { return kq * 32768; }
__host__ __device__ constexpr int l_wa(int kq) { return kq * 32768 + 16384; }
static_assert(L_GW + 1024 <= LDS_TOTAL && L_MISC + 256 <= LDS_TOTAL, "lds");
constexpr float QSCALE = 0.25f * 1.4426950408889634f;
constexpr unsigned SPIN_CAP = 1u << 20;

struct Params {
  const char* x; const char* wA; const char* woA; const char* wB; const char* woB; const char* gw; char* out; char* ws;
};

__device__ __forceinline__ unsigned cvtpk(float lo, float hi) {
  f2_t v = {lo, hi};
  return __builtin_bit_cast(unsigned, __builtin_convertvector(v, bf2_t));
}
__device__ __forceinline__ u32x4 pack8(const f32x16& a, int base, float s) {
  u32x4 r;
#pragma unroll
  for (int i = 0; i < 4; ++i) r[i] = cvtpk(a[base + 2 * i] * s, a[base + 2 * i + 1] * s);
  return r;
}
__device__ __forceinline__ u32x4 pack8n(const f32x16& a, int base) {
  u32x4 r;
#pragma unroll
  for (int i = 0; i < 4; ++i) r[i] = cvtpk(a[base + 2 * i], a[base + 2 * i + 1]);
  return r;
}
template <bool F> __device__ __forceinline__ void st16(__amdgpu_buffer_rsrc_t r, unsigned off, u32x4 v) {
  __builtin_amdgcn_raw_buffer_store_b128(v, r, off, 0, F ? 16 : 0);
}
template <bool F> __device__ __forceinline__ u32x4 ld16(__amdgpu_buffer_rsrc_t r, unsigned off) {
  return __builtin_amdgcn_raw_buffer_load_b128(r, off, 0, F ? 16 : 0);
}
__device__ __forceinline__ __amdgpu_buffer_rsrc_t mkrs(const void* p, unsigned bytes) {
  return __builtin_amdgcn_make_buffer_rsrc((void*)p, 0, bytes, 0x00020000);
}
template <class T> __device__ __forceinline__ LAS T* lp(unsigned a) { return (LAS T*)(uintptr_t)a; }
__device__ __forceinline__ bf16x8 lds_b128(unsigned a) { return *lp<const bf16x8>(a); }
__device__ __forceinline__ bf16x8 lds_tr2(unsigned a, unsigned second) {
  s16x4 lo = __builtin_amdgcn_ds_read_tr16_b64_v4i16(lp<s16x4>(a));
  s16x4 hi = __builtin_amdgcn_ds_read_tr16_b64_v4i16(lp<s16x4>(a + second));
  return __builtin_shufflevector(lo, hi, 0, 1, 2, 3, 4, 5, 6, 7);
}
__device__ __forceinline__ void glds16s(const void* sbase, unsigned voff, unsigned dst) {
  unsigned sv;
  asm volatile("s_mov_b32 %0, m0\n\ts_mov_b32 m0, %3\n\ts_nop 0\n\tglobal_load_lds_dwordx4 %1, %2\n\ts_mov_b32 m0, %0" : "=&s"(sv) : "v"(voff), "s"(sbase), "s"(dst) : "memory");
}
template <int N> __device__ __forceinline__ void waitv() { asm volatile("s_waitcnt vmcnt(%0)" :: "n"(N) : "memory"); }

#define STAMP(i)

__device__ __forceinline__ void poll_flag_at(char* ws, size_t off, int lane, u64 tag) {
  GAS u64* f = (GAS u64*)(ws + off);
  unsigned spins = 0;
  while (true) {
    u64 v = __hip_atomic_load(f, RLX_AGENT);
    if (__all(v == tag)) break;
    __builtin_amdgcn_s_sleep(1);
    if (++spins > SPIN_CAP) { if (lane == 0) __hip_atomic_store((GAS unsigned*)(ws + WS_TMO), 1u, RLX_AGENT); break; }
  }
}
__device__ __forceinline__ void poll_flags(char* ws, size_t off, int n, int lane, u64 tag) { poll_flag_at(ws, off + (lane < n ? lane : 0) * 8, lane, tag); }
__device__ __forceinline__ void set_flag(char* ws, size_t off, u64 tag) { __hip_atomic_store((GAS u64*)(ws + off), tag, RLX_AGENT); }

struct P1Loader {
  const char* xs; const char* wsrc; unsigned xo, wo; int lw, rot, halo_fix;
  __device__ __forceinline__ void init(const Params& p, int lw_, int lane, int br, int hq, int tb, int rot_) {
    lw = lw_; rot = rot_;
    const int qs = (lane & 7) ^ ((lane >> 4) & 3) ^ (4 * (lw & 1));
    xs = p.x + (ptrdiff_t)(tb * 128 - 32) * 1024;
    xo = (lane >> 3) * 1024 + qs * 16;
    halo_fix = tb == 0 ? 32 * 1024 : 0;
    wsrc = (br ? p.wB : p.wA) + (size_t)(8 * lw) * 3072 + hq * 128;
    wo = (lane >> 3) * 3072 + (((lane & 7) ^ (((lane >> 4) & 1) << 2)) * 16);
  }
  __device__ __forceinline__ void unit(int u, int slot, bool halo) {
    const int ku = (u + rot) & 7;
    const unsigned xd = L_XR + slot * XSLOT, wd = L_WR + slot * WSLOT + lw * 1024;
    const char* xu = xs + ku * 128;
    const char* wu = wsrc + (size_t)ku * 64 * 3072;
    glds16s(xu + (4 + lw) * 8192, xo, xd + (4 + lw) * 1024);
    glds16s(wu, wo, wd);
    glds16s(xu + (12 + lw) * 8192, xo, xd + (12 + lw) * 1024);
    glds16s(wu + 1024, wo, wd + 8192);
    glds16s(wu + 2048, wo, wd + 16384);
    if (halo) glds16s(xu + lw * 8192 + halo_fix, xo, xd + lw * 1024);
  }
};

__device__ __forceinline__ void local_unit(const Params& p, int hl, int tt, int h, int T, int lane) {
  const int r32 = lane & 31, hh = lane >> 5;
  const bf16x8 q = lds_b128(L_BQ + (hl * 4 + tt) * 1024 + lane * 16);
  const bf16x8 kp = lds_b128(L_BK + (hl * 5 + tt) * 1024 + lane * 16), kc = lds_b128(L_BK + (hl * 5 + tt + 1) * 1024 + lane * 16);
  const unsigned vo = L_BV + (hl * 5 + tt) * 1024 + hh * 256 + (r32 & 15) * 16;
  u32x4 vp = *lp<const u32x4>(vo + 512), v0 = *lp<const u32x4>(vo + 1024), v1 = *lp<const u32x4>(vo + 1536);
  if (r32 >= 16) {
    const unsigned o = (r32 == 16 || r32 == 20) ? 0x3F803F80u : 0u;
    v0 = u32x4{o, o, o, o}; v1 = v0; vp = v0;
  }
  u64 m = 0x5555ull << (18 + r32);
  if (T == 0) m &= 0xFFFFFFFF00000000ull;
  m = ~(m >> (4 * hh));
  const int mlo = (int)(unsigned)m, mhi = (int)(unsigned)(m >> 32);
  const int NEG = 0xf149f2ca;
  f32x16 cc, cp;
#pragma unroll
  for (int i = 0; i < 16; ++i) {
    const int b = (i & 3) + 8 * (i >> 2);
    cc[i] = __int_as_float(__builtin_amdgcn_sbfe(mhi, b, 1) & NEG);
    cp[i] = i >= 8 ? __int_as_float(__builtin_amdgcn_sbfe(mlo, b, 1) & NEG) : 0.f;
  }
  f32x16 sc = MFMA(kc, q, cc);
  f32x16 sp = MFMA(kp, q, cp);
  float mx = fmaxf(sc[0], sc[1]);
#pragma unroll
  for (int i = 2; i < 16; i += 2) mx = fmaxf(fmaxf(mx, sc[i]), sc[i + 1]);
#pragma unroll
  for (int i = 8; i < 16; i += 2) mx = fmaxf(fmaxf(mx, sp[i]), sp[i + 1]);
  {
    auto rr = __builtin_amdgcn_permlane32_swap(__float_as_uint(mx), __float_as_uint(mx), false, false);
    mx = fmaxf(__uint_as_float(rr[0]), __uint_as_float(rr[1]));
  }
  u32x4 p0, p1, pp;
#pragma unroll
  for (int i = 0; i < 4; ++i) {
    p0[i] = cvtpk(__builtin_amdgcn_exp2f(sc[2 * i] - mx), __builtin_amdgcn_exp2f(sc[2 * i + 1] - mx));
    p1[i] = cvtpk(__builtin_amdgcn_exp2f(sc[8 + 2 * i] - mx), __builtin_amdgcn_exp2f(sc[8 + 2 * i + 1] - mx));
    pp[i] = cvtpk(__builtin_amdgcn_exp2f(sp[8 + 2 * i] - mx), __builtin_amdgcn_exp2f(sp[8 + 2 * i + 1] - mx));
  }
  f32x16 o = {};
  o = MFMA(__builtin_bit_cast(bf16x8, v0), __builtin_bit_cast(bf16x8, p0), o);
  o = MFMA(__builtin_bit_cast(bf16x8, v1), __builtin_bit_cast(bf16x8, p1), o);
  o = MFMA(__builtin_bit_cast(bf16x8, vp), __builtin_bit_cast(bf16x8, pp), o);
  __builtin_amdgcn_raw_buffer_store_b128(pack8(o, 0, __builtin_amdgcn_rcpf(o[8])), mkrs(p.ws + WS_OB, FRAG_BYTES), (T * 32 + h) * 1024 + lane * 16, 0, 16);
}

template <bool F>
__device__ __forceinline__ void phase1(const Params& p, int wid, int lane, int br, int hq, int tb, int rot, int h2, u64 tagv) {
  const int r32 = lane & 31, hh = lane >> 5, q4 = (lane & 15) >> 2;
  const bool main = wid < 8, halo = br && wid >= 8 && wid < 12;
  const int tt = main ? (wid & 3) + 1 : 0, half = main ? wid >> 2 : (wid >> 1) & 1;
  const int row = 32 * tt + r32, f = (row >> 1) & 7;
  const unsigned xa0 = L_XR + row * 128 + (((hh ^ (f & 1)) | (f & 6)) * 16);
  const unsigned wa0 = L_WR + (8 * hh + q4) * 128 + ((half ^ ((q4 >> 1) & 1)) * 64) + ((lane >> 4) & 1) * 32 + (lane & 3) * 8;
  f32x16 aq = {}, ak = {}, av = {};
  if (main) {
    BAR();
    STAMP(1);
    unsigned xa[4], wa = wa0;
#pragma unroll
    for (int m = 0; m < 4; ++m) xa[m] = xa0 ^ (m << 5);
    OPAQUE(wa);
    bf16x8 xf[4], wq[4], wk[4], wv[4];
#pragma unroll
    for (int m = 0; m < 2; ++m) {
      SB(); xf[m] = lds_b128(xa[m]); wq[m] = lds_tr2(wa + m * 2048, 512);
      SB(); wk[m] = lds_tr2(wa + m * 2048 + 8192, 512);
      SB(); wv[m] = lds_tr2(wa + m * 2048 + 16384, 512);
    }
    SB();
    int slot = 0;
#pragma unroll UNR_C
    for (int u = 0; u < 8; ++u) {
      const int nslot = slot == 2 ? 0 : slot + 1;
      const int dx = (nslot - slot) * XSLOT, dw = (nslot - slot) * WSLOT;
#pragma unroll
      for (int m = 0; m < 4; ++m) {
        if (m == 2) {
          wa += dw;
#pragma unroll
          for (int i = 0; i < 4; ++i) xa[i] += dx;
        }
        const int mn = (m + 2) & 3;
        SB();
        aq = MFMA(wq[m], xf[m], aq);
        SB();
        xf[mn] = lds_b128(xa[mn]); wq[mn] = lds_tr2(wa + mn * 2048, 512);
        SB();
        ak = MFMA(wk[m], xf[m], ak);
        SB();
        wk[mn] = lds_tr2(wa + mn * 2048 + 8192, 512);
        SB();
        av = MFMA(xf[m], wv[m], av);
        SB();
        wv[mn] = lds_tr2(wa + mn * 2048 + 16384, 512);
        if (m & 1) BAR();
      }
      slot = nslot;
    }
  } else if (halo) {
    SETP(3);
    P1Loader L; L.init(p, wid - 8, lane, br, hq, tb, rot);
    L.unit(0, 0, true); L.unit(1, 1, true);
    waitv<6>();
    BAR();
    auto halo_loop = [&](auto ISV) {
      constexpr bool isv = decltype(ISV)::value;
      unsigned xa[4], wa = wa0 + (isv ? 16384 : 8192);
#pragma unroll
      for (int m = 0; m < 4; ++m) xa[m] = xa0 ^ (m << 5);
      OPAQUE(wa);
      bf16x8 xf[2], wf[2];
      f32x16 a = {};
      xf[0] = lds_b128(xa[0]); wf[0] = lds_tr2(wa, 512);
      int slot = 0;
#pragma unroll UNR_L
      for (int u = 0; u < 8; ++u) {
        const int nslot = slot == 2 ? 0 : slot + 1;
        const int dx = (nslot - slot) * XSLOT, dw = (nslot - slot) * WSLOT;
        if (u + 2 < 8) L.unit(u + 2, nslot == 2 ? 0 : nslot + 1, true);
#pragma unroll
        for (int m = 0; m < 4; ++m) {
          const int c = m & 1, n = c ^ 1;
          if (m == 3) {
            wa += dw;
#pragma unroll
            for (int i = 0; i < 4; ++i) xa[i] += dx;
          }
          const int mn = (m + 1) & 3;
          SB();
          if (isv) a = MFMA(xf[c], wf[c], a); else a = MFMA(wf[c], xf[c], a);
          SB();
          xf[n] = lds_b128(xa[mn]); wf[n] = lds_tr2(wa + mn * 2048, 512);
          if (m == 1) { if (u + 2 < 8) waitv<6>(); else waitv<0>(); }
          if (m & 1) BAR();
        }
        slot = nslot;
      }
      return a;
    };
    if (wid & 1) av = halo_loop(std::true_type{}); else ak = halo_loop(std::false_type{});
  } else {
    SETP(3);
    P1Loader L; L.init(p, wid - 8, lane, br, hq, tb, rot);
    const bool gatew = !br && wid == 8;
    if (gatew) glds16s(p.gw, lane * 16, L_GW);
    L.unit(0, 0, false); L.unit(1, 1, false);
    waitv<5>();
    BAR();
    float ga = 0.f;
    const int grow = 32 + 16 * hq + (lane >> 2), gf = (grow >> 1) & 7;
    int slot = 0;
#pragma unroll UNR_L
    for (int u = 0; u < 8; ++u) {
      const int nslot = slot == 2 ? 0 : slot + 1;
      if (u + 2 < 8) L.unit(u + 2, nslot == 2 ? 0 : nslot + 1, false);
      if (gatew) {
#pragma unroll
        for (int e = 0; e < 2; ++e) {
          const int q = 2 * (lane & 3) + e;
          const u32x4 xv = *lp<const u32x4>(L_XR + slot * XSLOT + grow * 128 + ((q ^ gf) * 16));
          const u32x4 gv = *lp<const u32x4>(L_GW + ((u + rot) & 7) * 128 + q * 16);
#pragma unroll
          for (int d = 0; d < 4; ++d) {
            const unsigned xa = xv[d], gb = gv[d];
            ga = __builtin_amdgcn_fdot2_f32_bf16(__builtin_bit_cast(bf2_t, xa), __builtin_bit_cast(bf2_t, gb), ga, false);
          }
        }
      }
      if (u + 2 < 8) waitv<5>(); else waitv<0>();
      BAR();
      BAR();
      slot = nslot;
    }
    if (gatew) {
      ga += __shfl_xor(ga, 1); ga += __shfl_xor(ga, 2);
      const float g = 1.f / (1.f + __expf(-ga));
      if ((lane & 3) == 0) __hip_atomic_store((GAS float*)(p.ws + WS_G) + tb * 128 + hq * 16 + (lane >> 2), g, RLX_AGENT);
    }
  }
  SETP(0);
  STAMP(2);
  const int h0 = 2 * half;
  GAS u64* fa1 = (GAS u64*)(p.ws + WS_FA1) + (h2 >> 1) * 64 + lane;
  if (!br) {
    if (main) {
      const int T = tb * 4 + tt - 1, hg = 4 * hq + h0;
      auto rq = mkrs(p.ws + WS_QA, FRAG_BYTES), rk = mkrs(p.ws + WS_KA, FRAG_BYTES), rv = mkrs(p.ws + WS_VA, FRAG_BYTES);
      const unsigned o0 = (hg * 64 + T) * 1024 + lane * 16;
      st16<F>(rk, o0, pack8n(ak, 0));
      st16<F>(rk, o0 + 65536, pack8n(ak, 8));
      const unsigned ov = ((hg + ((lane >> 4) & 1)) * 64 + T) * 1024 + hh * 256 + (lane & 15) * 16;
      st16<F>(rv, ov, pack8n(av, 0));
      st16<F>(rv, ov + 512, pack8n(av, 8));
      st16<F>(rq, o0, pack8(aq, 0, QSCALE));
      st16<F>(rq, o0 + 65536, pack8(aq, 8, QSCALE));
      STAMP(3);
      if (F) {
        WAITV(0);
        STAMP(5);
        if (wid != 0 && lane == 0) set_flag(p.ws, WS_FA1 + ((hg >> 1) * 64 + T) * 8, tagv);
      }
    }
    if (F) {
      if (wid == 8) WAITV(0);
      BAR();
      STAMP(6);
      if (wid == 0 && lane == 0) set_flag(p.ws, WS_FA1 + ((2 * hq) * 64 + tb * 4) * 8, tagv);
      if (wid == 15) poll_flag_at(p.ws, (char*)fa1 - p.ws, lane, tagv);
      STAMP(7);
      BAR();
    }
  } else {
    if (main || (halo && !(wid & 1))) {
      LAS u32x4* kd = lp<u32x4>(L_BK + (h0 * 5 + tt) * 1024 + lane * 16);
      kd[0] = pack8n(ak, 0); kd[5 * 64] = pack8n(ak, 8);
    }
    if (main || (halo && (wid & 1))) {
      LAS u32x4* vd = lp<u32x4>(L_BV + ((h0 + ((lane >> 4) & 1)) * 5 + tt) * 1024 + hh * 256 + (lane & 15) * 16);
      vd[0] = pack8n(av, 0); vd[32] = pack8n(av, 8);
    }
    if (main) {
      LAS u32x4* qd = lp<u32x4>(L_BQ + (h0 * 4 + tt - 1) * 1024 + lane * 16);
      qd[0] = pack8(aq, 0, QSCALE); qd[4 * 64] = pack8(aq, 8, QSCALE);
    }
    STAMP(3);
    u64 seen = 0;
    WAITL0();
    BAR();
    if (F && wid == 15) seen = __hip_atomic_load(fa1, RLX_AGENT);
    STAMP(5);
    local_unit(p, wid >> 2, wid & 3, 4 * hq + (wid >> 2), tb * 4 + (wid & 3), lane);
    STAMP(6);
    if (F) {
      if (wid == 15 && !__all(seen == tagv)) poll_flag_at(p.ws, (char*)fa1 - p.ws, lane, tagv);
      STAMP(7);
      WAITL0();
      BAR();
    }
  }
}

__device__ __forceinline__ void attn_tile(unsigned ka, unsigned va, int off, const bf16x8& qf, f32x16& o, const f32x16& c) {
  bf16x8 kf = lds_b128(ka + off);
  bf16x8 v0 = lds_b128(va + off);
  bf16x8 v1 = lds_b128(va + off + 512);
  f32x16 s = MFMA(kf, qf, c);
  u32x4 p0, p1;
#pragma unroll
  for (int i = 0; i < 4; ++i) p0[i] = cvtpk(__builtin_amdgcn_exp2f(s[2 * i]), __builtin_amdgcn_exp2f(s[2 * i + 1]));
#pragma unroll
  for (int i = 0; i < 4; ++i) p1[i] = cvtpk(__builtin_amdgcn_exp2f(s[8 + 2 * i]), __builtin_amdgcn_exp2f(s[8 + 2 * i + 1]));
  o = MFMA(v0, __builtin_bit_cast(bf16x8, p0), o);
  o = MFMA(v1, __builtin_bit_cast(bf16x8, p1), o);
}

template <bool F>
__device__ __forceinline__ void phase23(const Params& p, int wid, int lane, int h, int j, int tb3, int cb, u64 tag, int PH) {
  const int qt = wid & 7, kh = wid >> 3, r32 = lane & 31, hh = lane >> 5;
  LAS float* red = lp<float>(L_RED);
  const int tt = qt & 1, kq = qt >> 1;
  const int T = tb3 * 2 + tt;
  const int q4 = (lane & 15) >> 2;
  u32x4 of[8];
  float gate = 0.f;
  unsigned warm0 = 0, warm1 = 0;
  const size_t obf = (lane & 2) ? WS_FA1 + ((4 * (T & 3) + 2 * (lane & 1)) * 64 + (T & ~3)) * 8 : WS_FB2 + ((tb3 >> 1) * 8 + kq * 2 + (lane & 1)) * 8;
  auto load_ob = [&]() {
    auto r = mkrs(p.ws + WS_OB, FRAG_BYTES);
#pragma unroll
    for (int i = 0; i < 8; ++i) of[i] = ld16<F>(r, (T * 32 + kq * 8 + i) * 1024 + lane * 16);
    gate = __hip_atomic_load((GAS float*)(p.ws + WS_G) + T * 32 + r32, RLX_AGENT);
  };
  if (PH != 3) {
    auto rq = mkrs(p.ws + WS_QA + (size_t)h * 65536, 65536), rk = mkrs(p.ws + WS_KA + (size_t)h * 65536, 65536), rv = mkrs(p.ws + WS_VA + (size_t)h * 65536, 65536);
    u32x4 pc[8];
    const int t0 = (wid & 3) + ((wid >> 2) & 1) * 32;
    pc[0] = ld16<F>(kh ? rv : rk, ((t0 + 4 * j) & 63) * 1024 + lane * 16);
    const bf16x8 qf = __builtin_bit_cast(bf16x8, ld16<F>(rq, (j * 8 + qt) * 1024 + lane * 16));
    {
      const unsigned v = (lane >> 2) == 0 || (lane >> 2) == 4 ? 0x3F803F80u : 0u;
      *lp<unsigned>(L_ONE + wid * 256 + lane * 4) = v;
      if (wid == 0 && lane < 2) *lp<unsigned>(L_MISC + lane * 4) = 0u;
    }
    unsigned kaddr = L_K + kh * 32768 + lane * 16, vaddr, vstep;
    if (r32 < 16) { vaddr = L_V + kh * 32768 + hh * 256 + r32 * 16; vstep = 2048; }
    else { vaddr = L_ONE + hh * 256 + ((r32 >= 20 && r32 < 28) ? 64 : 0) + ((r32 == 16 || r32 == 20) ? 0 : 16); vstep = 0; }
    const unsigned kaddr0 = kaddr, vaddr0 = vaddr;
    const unsigned pdb = ((wid & 1) ? L_V : L_K) + lane * 16;
    auto tile_of = [&](int n) { const int ti = ((n - 1) * 16 + wid) >> 1; return ti < 28 ? 4 + ti : 8 + ti; };
    auto load_rest = [&](int n0, int n1) {
#pragma unroll
      for (int n = 1; n < 8; ++n) if (n >= n0 && n < n1) pc[n] = ld16<F>((wid & 1) ? rv : rk, ((tile_of(n) + 4 * j) & 63) * 1024 + lane * 16);
    };
    auto put = [&](int n) { *lp<u32x4>(pdb + tile_of(n) * 1024) = pc[n]; };
    f32x16 o = {};
    const f32x16 zero = {};
    BAR();
    load_rest(1, 4);
    STAMP(9);
    *lp<u32x4>((kh ? L_V : L_K) + t0 * 1024 + lane * 16) = pc[0];
    WAITL0();
    BAR();
    STAMP(10);
    SETP(3); attn_tile(kaddr, vaddr, 0, qf, o, zero);
    SB();
    load_rest(4, 8);
    put(1); put(2);
    SB();
    SETP(2); attn_tile(kaddr, vaddr, 1024, qf, o, zero);
    put(3);
    kaddr += 2048; vaddr += vstep;
    SETP(1); attn_tile(kaddr, vaddr, 0, qf, o, zero);
    put(4); put(5);
    SETP(0); attn_tile(kaddr, vaddr, 1024, qf, o, zero);
    put(6); put(7);
    kaddr += 2048; vaddr += vstep;
    if (F) WAITV(0);
    WAITL0();
    STAMP(11);
    BAR();
    if (F && wid == 0 && lane == 0) {
      const int b = blockIdx.x, xcd = b & 7, idx = b >> 3;
      if ((idx >> 3) & 1) set_flag(p.ws, WS_FB2 + (((xcd & 1) * 8 + (idx & 7)) * 8 + (xcd >> 1) * 2 + (idx >> 4)) * 8, tag);
    }
    if (F && kh == 1) {
      const char* a0 = p.woA + (size_t)(qt * 64 + lane) * 1024 + cb * 128;
      const char* a1 = p.woB + (size_t)(qt * 64 + lane) * 1024 + cb * 128;
      asm volatile("global_load_dword %0, %2, off\n\tglobal_load_dword %1, %3, off" : "=&v"(warm0), "=&v"(warm1) : "v"(a0), "v"(a1) : "memory");
    }
    SETP(3);
    const unsigned so = (lane >> 3) * 1024 + (((lane & 7) ^ (((lane >> 4) & 1) << 2)) * 16);
    auto load_wo = [&](const char* wo, unsigned dst) {
#pragma unroll
      for (int n = 0; n < 8; ++n) glds16s(wo + (size_t)(kq * 128 + tt * 64 + n * 8) * 1024 + cb * 128, so, dst + (tt * 64 + n * 8) * 128);
    };
    vstep *= 2;
    auto quads = [&](int n) {
#pragma unroll 1
      for (int it = 0; it < n; ++it) {
        attn_tile(kaddr, vaddr, 0, qf, o, zero);
        attn_tile(kaddr, vaddr, 1024, qf, o, zero);
        attn_tile(kaddr, vaddr, 2048, qf, o, zero);
        attn_tile(kaddr, vaddr, 3072, qf, o, zero);
        kaddr += 4096; vaddr += vstep;
      }
    };
    quads(3);
    if (lane == 0) __hip_atomic_fetch_add(lp<unsigned>(L_MISC + 4), 1u, __ATOMIC_RELAXED, __HIP_MEMORY_SCOPE_WORKGROUP);
    if (F && kh == 1) {
      poll_flag_at(p.ws, obf, lane, tag);
      load_ob();
    }
    quads(1);
    if (F && kh == 1) {
      while (*lp<volatile unsigned>(L_MISC + 4) != 16u) __builtin_amdgcn_s_sleep(1);
      load_wo(p.woB, l_wb(kq));
    }
    quads(1);
    SETP(2);
    quads(1);
    SETP(1);
    attn_tile(kaddr, vaddr, 0, qf, o, zero);
    attn_tile(kaddr, vaddr, 1024, qf, o, zero);
    attn_tile(kaddr, vaddr, 2048, qf, o, zero);
    SETP(0);
    attn_tile(kaddr, vaddr, 3072, qf, o, zero);
    vstep /= 2;
    STAMP(13);
    auto rO = mkrs(p.ws + WS_OA, FRAG_BYTES);
    const unsigned oo = ((j * 8 + qt) * 32 + h) * 1024 + lane * 16;
    if (kh == 1) {
#pragma unroll
      for (int i = 0; i < 9; ++i) red[(qt * 9 + i) * 64 + lane] = o[i];
      WAITV(0);
      asm volatile("" :: "v"(warm0), "v"(warm1));
    }
    WAITL0();
    BAR();
    if (kh == 0) {
      bool ok = true;
#pragma unroll
      for (int i = 0; i < 9; ++i) { o[i] += red[(qt * 9 + i) * 64 + lane]; ok = ok && (fabsf(o[i]) <= 3.0e38f); }
      ok = ok && (o[8] >= 8.67e-19f);
      if (__all(ok)) st16<F>(rO, oo, pack8(o, 0, __builtin_amdgcn_rcpf(o[8])));
      else if (lane == 0) *lp<unsigned>(L_MISC) = 1u;
    }
    WAITL0();
    BAR();
    if (*lp<volatile unsigned>(L_MISC)) {
      if (F) {
        for (int n = 0; n < 8; ++n) {
          const int t = n * 8 + qt;
          *lp<u32x4>((kh ? L_V : L_K) + t * 1024 + lane * 16) = ld16<F>(kh ? rv : rk, ((t + 4 * j) & 63) * 1024 + lane * 16);
        }
        WAITL0(); BAR();
      }
      float mx = -3.0e38f;
      for (int t = 0; t < 32; ++t) {
        f32x16 s = MFMA(lds_b128(kaddr0 + t * 1024), qf, zero);
#pragma unroll
        for (int i = 0; i < 16; ++i) mx = fmaxf(mx, s[i]);
      }
      { auto rr = __builtin_amdgcn_permlane32_swap(__float_as_uint(mx), __float_as_uint(mx), false, false);
        mx = fmaxf(__uint_as_float(rr[0]), __uint_as_float(rr[1])); }
      red[wid * 64 + lane] = mx;
      WAITL0(); BAR();
      mx = fmaxf(mx, red[(wid ^ 8) * 64 + lane]);
      WAITL0(); BAR();
      f32x16 c;
#pragma unroll
      for (int i = 0; i < 16; ++i) { c[i] = -mx; o[i] = 0.f; }
      unsigned va = vaddr0;
      for (int t = 0; t < 32; ++t) { attn_tile(kaddr0 + t * 1024, va, 0, qf, o, c); va += vstep >> 1; }
      if (kh == 1) {
#pragma unroll
        for (int i = 0; i < 9; ++i) red[(qt * 9 + i) * 64 + lane] = o[i];
      }
      WAITL0(); BAR();
      if (kh == 0) {
#pragma unroll
        for (int i = 0; i < 9; ++i) o[i] += red[(qt * 9 + i) * 64 + lane];
        st16<F>(rO, oo, pack8(o, 0, __builtin_amdgcn_rcpf(o[8])));
      }
      WAITL0(); BAR();
      if (F && kh == 1) { load_ob(); load_wo(p.woB, l_wb(kq)); WAITV(0); }
      BAR();
    }
    if (F && kh == 1) load_wo(p.woA, l_wa(kq));
    if (F && kh == 0) {
      WAITV(0);
      if (lane == 0) set_flag(p.ws, WS_FA2 + ((j * 8 + qt) * 32 + h) * 8, tag);
    }
    STAMP(14);
  }
  if (PH == 2) return;
  const unsigned wa = (4 * hh + q4) * 128 + ((lane >> 4) & 1) * 32 + (lane & 3) * 8;
  const unsigned sw = ((q4 >> 1) & 1) * 64;
  f32x16 b0 = {}, b1 = {}, a0 = {}, a1 = {};
  if (!F) {
    if (kh == 1) {
      const unsigned so = (lane >> 3) * 1024 + (((lane & 7) ^ (((lane >> 4) & 1) << 2)) * 16);
#pragma unroll
      for (int n = 0; n < 8; ++n) glds16s(p.woB + (size_t)(kq * 128 + tt * 64 + n * 8) * 1024 + cb * 128, so, l_wb(kq) + (tt * 64 + n * 8) * 128);
#pragma unroll
      for (int n = 0; n < 8; ++n) glds16s(p.woA + (size_t)(kq * 128 + tt * 64 + n * 8) * 1024 + cb * 128, so, l_wa(kq) + (tt * 64 + n * 8) * 128);
      load_ob();
      WAITV(0);
    }
    BAR();
  }
  if (kh == 1) {
    STAMP(18);
#pragma unroll
    for (int i = 0; i < 8; ++i) {
      b0 = MFMA(lds_tr2(l_wb(0) + kq * 32768 + wa + sw + i * 2048, 1024), __builtin_bit_cast(bf16x8, of[i]), b0);
      b1 = MFMA(lds_tr2(l_wb(0) + kq * 32768 + wa + (sw ^ 64) + i * 2048, 1024), __builtin_bit_cast(bf16x8, of[i]), b1);
    }
    STAMP(19);
    if (F) poll_flags(p.ws, WS_FA2 + (T * 32 + kq * 8) * 8, 8, lane, tag);
    {
      auto r = mkrs(p.ws + WS_OA, FRAG_BYTES);
#pragma unroll
      for (int i = 0; i < 8; ++i) of[i] = ld16<F>(r, (T * 32 + kq * 8 + i) * 1024 + lane * 16);
    }
    WAITV(8);
    STAMP(20);
  }
  BAR();
  using f32x4 = __attribute__((ext_vector_type(4))) float;
  auto exb = [&](int ow) { return lp<f32x4>((2 * tt + (ow >> 1)) * 32768 + (ow & 1) * 8192 + lane * 16); };
  if (kh == 1) {
#pragma unroll
    for (int i = 0; i < 8; ++i) {
      a0 = MFMA(lds_tr2(l_wa(0) + kq * 32768 + wa + sw + i * 2048, 1024), __builtin_bit_cast(bf16x8, of[i]), a0);
      a1 = MFMA(lds_tr2(l_wa(0) + kq * 32768 + wa + (sw ^ 64) + i * 2048, 1024), __builtin_bit_cast(bf16x8, of[i]), a1);
    }
#pragma unroll
    for (int i = 0; i < 16; ++i) { a0[i] = gate * a0[i] + (1.f - gate) * b0[i]; a1[i] = gate * a1[i] + (1.f - gate) * b1[i]; }
    STAMP(21);
#pragma unroll
    for (int ow = 0; ow < 4; ++ow) {
#pragma unroll
      for (int e = 0; e < 2; ++e) {
        const int i = 8 * (ow >> 1) + 4 * e;
        exb(ow)[(kq * 2 + e) * 64] = (ow & 1) ? f32x4{a1[i], a1[i + 1], a1[i + 2], a1[i + 3]} : f32x4{a0[i], a0[i + 1], a0[i + 2], a0[i + 3]};
      }
    }
    WAITL0();
  }
  BAR();
  if (kh == 1) {
    float s[8];
    LAS f32x4* ex = lp<f32x4>((2 * tt + (kq >> 1)) * 32768 + (kq & 1) * 8192 + lane * 16);
#pragma unroll
    for (int e = 0; e < 2; ++e) {
      const f32x4 v = (ex[(0 * 2 + e) * 64] + ex[(1 * 2 + e) * 64]) + (ex[(2 * 2 + e) * 64] + ex[(3 * 2 + e) * 64]);
      s[4 * e] = v[0]; s[4 * e + 1] = v[1]; s[4 * e + 2] = v[2]; s[4 * e + 3] = v[3];
    }
    const unsigned w0 = cvtpk(s[0], s[1]), w1 = cvtpk(s[2], s[3]), w2 = cvtpk(s[4], s[5]), w3 = cvtpk(s[6], s[7]);
    auto a = __builtin_amdgcn_permlane32_swap(w0, w2, false, false);
    auto b = __builtin_amdgcn_permlane32_swap(w1, w3, false, false);
    *(GAS u32x4*)(p.out + (size_t)(T * 32 + r32) * 1024 + cb * 128 + (kq & 1) * 64 + (kq >> 1) * 32 + hh * 16) = u32x4{a[0], b[0], a[1], b[1]};
  }
}

template <int PH>
__global__ __launch_bounds__(1024) void gated_mix_kernel(Params p) {
  const int wid = __builtin_amdgcn_readfirstlane(threadIdx.x >> 6);
  const int lane = __builtin_amdgcn_mbcnt_hi(~0u, __builtin_amdgcn_mbcnt_lo(~0u, 0u));
  const int b = blockIdx.x, xcd = b & 7, idx = b >> 3;
  u64 tag = 0;
  if (PH == 0) tag = __hip_atomic_load((GAS u64*)(p.ws + WS_NONCE), RLX_AGENT) + 1;
  STAMP(0 + (PH >= 2 ? (PH - 1) * 8 : 0));
  if (PH == 0 || PH == 1) phase1<PH == 0>(p, wid, lane, (idx >> 3) & 1, (xcd >> 1) * 2 + (idx >> 4), (xcd & 1) * 8 + (idx & 7), ((idx & 7) + 2 * (idx >> 3)) & 7, xcd * 4 + (idx >> 3), tag);
  if (PH == 0 || PH == 1) STAMP(4);
  tag = ((u64)__builtin_amdgcn_readfirstlane((unsigned)(tag >> 32)) << 32) | (unsigned)__builtin_amdgcn_readfirstlane((unsigned)tag);
  if (PH != 1) phase23<PH == 0>(p, wid, lane, xcd * 4 + (idx >> 3), idx & 7, xcd * 4 + (idx >> 3), idx & 7, tag, PH);
  STAMP(23);
  if (PH == 0 && b == 0 && wid == 8 && lane == 0) set_flag(p.ws, WS_NONCE, tag);
}

extern "C" void kernel_launch(void* const* d_in, const int* in_sizes, int n_in, void* d_out, int out_size, void* d_ws, size_t ws_size,
                              hipStream_t stream) {
  Params p;
  p.x = (const char*)d_in[0]; p.wA = (const char*)d_in[1]; p.woA = (const char*)d_in[2];
  p.wB = (const char*)d_in[4]; p.woB = (const char*)d_in[5]; p.gw = (const char*)d_in[6];
  p.out = (char*)d_out; p.ws = (char*)d_ws;
  (void)hipFuncSetAttribute((const void*)gated_mix_kernel<0>, hipFuncAttributeMaxDynamicSharedMemorySize, LDS_TOTAL);
  gated_mix_kernel<0><<<256, 1024, LDS_TOTAL, stream>>>(p);
}
```

```cpp
#include <hip/hip_runtime.h>
#include <hip/hip_bf16.h>
#include <stdint.h>
#include <type_traits>

#define UNR_C 8
#define UNR_L 8
#define FUSE 1

typedef unsigned long long u64;
using f32x16 = __attribute__((ext_vector_type(16))) float;
using bf16x8 = __attribute__((ext_vector_type(8))) short;
using s16x4 = __attribute__((ext_vector_type(4))) short;
using u32x4 = __attribute__((ext_vector_type(4))) unsigned;
typedef __bf16 bf2_t __attribute__((ext_vector_type(2)));
typedef float f2_t __attribute__((ext_vector_type(2)));
#define GAS __attribute__((address_space(1)))
#define LAS __attribute__((address_space(3)))
#define MFMA(a, b, c) __builtin_amdgcn_mfma_f32_32x32x16_bf16((a), (b), (c), 0, 0, 0)
#define RLX_AGENT __ATOMIC_RELAXED, __HIP_MEMORY_SCOPE_AGENT
#define BAR() asm volatile("s_barrier" ::: "memory")
#define WAITV(n) asm volatile("s_waitcnt vmcnt(" #n ")" ::: "memory")
#define WAITL0() asm volatile("s_waitcnt lgkmcnt(0)" ::: "memory")
#define SETP(x) __builtin_amdgcn_s_setprio(x)
#define SB() __builtin_amdgcn_sched_barrier(0)
#define OPAQUE(x) asm volatile("" : "+v"(x))

constexpr size_t MB = 1u << 20;
constexpr size_t WS_NONCE = 0, WS_TMO = 64;
constexpr size_t WS_FA1 = 4096;
constexpr size_t WS_FB2 = 16384;
constexpr size_t WS_FG = 17408;
constexpr size_t WS_FA2 = 20480;
constexpr size_t WS_G = 40960;
constexpr size_t WS_QA = 1 * MB, WS_KA = 3 * MB, WS_VA = 5 * MB, WS_OA = 7 * MB, WS_OB = 9 * MB;
constexpr unsigned FRAG_BYTES = 2u << 20;
constexpr int LDS_TOTAL = 160 * 1024;
constexpr int L_XR = 0, XSLOT = 160 * 128, L_WR = 3 * XSLOT, WSLOT = 3 * 64 * 128, L_GW = L_WR + 3 * WSLOT;
constexpr int L_BK = 0, L_BV = 20480, L_BQ = 40960;
constexpr int L_K = 0, L_V = 65536, L_ONE = 131072, L_RED = L_ONE + 4096, L_MISC = L_RED + 8 * 9 * 256;
__host__ __device__ constexpr int l_wb(int kq) { return kq * 32768; }
__host__ __device__ constexpr int l_wa(int kq) { return kq * 32768 + 16384; }
static_assert(L_GW + 1024 <= LDS_TOTAL && L_MISC + 256 <= LDS_TOTAL, "lds");
constexpr float QSCALE = 0.25f * 1.4426950408889634f;
constexpr unsigned SPIN_CAP = 1u << 20;
constexpr int CODE_LINES = 256;

struct Params {
  const char* x; const char* wA; const char* woA; const char* wB; const char* woB; const char* gw; char* out; char* ws;
};

__device__ __forceinline__ unsigned cvtpk(float lo, float hi) {
  f2_t v = {lo, hi};
  return __builtin_bit_cast(unsigned, __builtin_convertvector(v, bf2_t));
}
__device__ __forceinline__ u32x4 pack8(const f32x16& a, int base, float s) {
  u32x4 r;
#pragma unroll
  for (int i = 0; i < 4; ++i) r[i] = cvtpk(a[base + 2 * i] * s, a[base + 2 * i + 1] * s);
  return r;
}
__device__ __forceinline__ u32x4 pack8n(const f32x16& a, int base) {
  u32x4 r;
#pragma unroll
  for (int i = 0; i < 4; ++i) r[i] = cvtpk(a[base + 2 * i], a[base + 2 * i + 1]);
  return r;
}
template <bool F> __device__ __forceinline__ void st16(__amdgpu_buffer_rsrc_t r, unsigned off, u32x4 v) {
  __builtin_amdgcn_raw_buffer_store_b128(v, r, off, 0, F ? 16 : 0);
}
template <bool F> __device__ __forceinline__ u32x4 ld16(__amdgpu_buffer_rsrc_t r, unsigned off) {
  return __builtin_amdgcn_raw_buffer_load_b128(r, off, 0, F ? 16 : 0);
}
__device__ __forceinline__ __amdgpu_buffer_rsrc_t mkrs(const void* p, unsigned bytes) {
  return __builtin_amdgcn_make_buffer_rsrc((void*)p, 0, bytes, 0x00020000);
}
template <class T> __device__ __forceinline__ LAS T* lp(unsigned a) { return (LAS T*)(uintptr_t)a; }
__device__ __forceinline__ bf16x8 lds_b128(unsigned a) { return *lp<const bf16x8>(a); }
__device__ __forceinline__ bf16x8 lds_tr2(unsigned a, unsigned second) {
  s16x4 lo = __builtin_amdgcn_ds_read_tr16_b64_v4i16(lp<s16x4>(a));
  s16x4 hi = __builtin_amdgcn_ds_read_tr16_b64_v4i16(lp<s16x4>(a + second));
  return __builtin_shufflevector(lo, hi, 0, 1, 2, 3, 4, 5, 6, 7);
}
__device__ __forceinline__ void glds16s(const void* sbase, unsigned voff, unsigned dst) {
  unsigned sv;
  asm volatile("s_mov_b32 %0, m0\n\ts_mov_b32 m0, %3\n\ts_nop 0\n\tglobal_load_lds_dwordx4 %1, %2\n\ts_mov_b32 m0, %0" : "=&s"(sv) : "v"(voff), "s"(sbase), "s"(dst) : "memory");
}
template <int N> __device__ __forceinline__ void waitv() { asm volatile("s_waitcnt vmcnt(%0)" :: "n"(N) : "memory"); }

#define STAMP(i)

__device__ __forceinline__ void poll_flag_at(char* ws, size_t off, int lane, u64 tag) {
  GAS u64* f = (GAS u64*)(ws + off);
  unsigned spins = 0;
  while (true) {
    u64 v = __hip_atomic_load(f, RLX_AGENT);
    if (__all(v == tag)) break;
    __builtin_amdgcn_s_sleep(1);
    if (++spins > SPIN_CAP) { if (lane == 0) __hip_atomic_store((GAS unsigned*)(ws + WS_TMO), 1u, RLX_AGENT); break; }
  }
  asm volatile("" ::: "memory");
}
__device__ __forceinline__ void spin_lds(unsigned a, unsigned v) {
  while (*(LAS volatile unsigned*)(uintptr_t)a != v) __builtin_amdgcn_s_sleep(1);
  asm volatile("" ::: "memory");
}
__device__ __forceinline__ void poll_flags(char* ws, size_t off, int n, int lane, u64 tag) { poll_flag_at(ws, off + (lane < n ? lane : 0) * 8, lane, tag); }
__device__ __forceinline__ void set_flag(char* ws, size_t off, u64 tag) { __hip_atomic_store((GAS u64*)(ws + off), tag, RLX_AGENT); }

struct P1Loader {
  const char* xs; const char* wsrc; unsigned xo, wo; int lw, rot, halo_fix;
  __device__ __forceinline__ void init(const Params& p, int lw_, int lane, int br, int hq, int tb, int rot_) {
    lw = lw_; rot = rot_;
    const int qs = (lane & 7) ^ ((lane >> 4) & 3) ^ (4 * (lw & 1));
    xs = p.x + (ptrdiff_t)(tb * 128 - 32) * 1024;
    xo = (lane >> 3) * 1024 + qs * 16;
    halo_fix = tb == 0 ? 32 * 1024 : 0;
    wsrc = (br ? p.wB : p.wA) + (size_t)(8 * lw) * 3072 + hq * 128;
    wo = (lane >> 3) * 3072 + (((lane & 7) ^ (((lane >> 4) & 1) << 2)) * 16);
  }
  __device__ __forceinline__ void unit(int u, int slot, bool halo) {
    const int ku = (u + rot) & 7;
    const unsigned xd = L_XR + slot * XSLOT, wd = L_WR + slot * WSLOT + lw * 1024;
    const char* xu = xs + ku * 128;
    const char* wu = wsrc + (size_t)ku * 64 * 3072;
    glds16s(xu + (4 + lw) * 8192, xo, xd + (4 + lw) * 1024);
    glds16s(wu, wo, wd);
    glds16s(xu + (12 + lw) * 8192, xo, xd + (12 + lw) * 1024);
    glds16s(wu + 1024, wo, wd + 8192);
    glds16s(wu + 2048, wo, wd + 16384);
    if (halo) glds16s(xu + lw * 8192 + halo_fix, xo, xd + lw * 1024);
  }
};

__device__ __forceinline__ void local_unit(const Params& p, int hl, int tt, int h, int T, int lane) {
  const int r32 = lane & 31, hh = lane >> 5;
  const bf16x8 q = lds_b128(L_BQ + (hl * 4 + tt) * 1024 + lane * 16);
  const bf16x8 kp = lds_b128(L_BK + (hl * 5 + tt) * 1024 + lane * 16), kc = lds_b128(L_BK + (hl * 5 + tt + 1) * 1024 + lane * 16);
  const unsigned vo = L_BV + (hl * 5 + tt) * 1024 + hh * 256 + (r32 & 15) * 16;
  u32x4 vp = *lp<const u32x4>(vo + 512), v0 = *lp<const u32x4>(vo + 1024), v1 = *lp<const u32x4>(vo + 1536);
  if (r32 >= 16) {
    const unsigned o = (r32 == 16 || r32 == 20) ? 0x3F803F80u : 0u;
    v0 = u32x4{o, o, o, o}; v1 = v0; vp = v0;
  }
  u64 m = 0x5555ull << (18 + r32);
  if (T == 0) m &= 0xFFFFFFFF00000000ull;
  m = ~(m >> (4 * hh));
  const int mlo = (int)(unsigned)m, mhi = (int)(unsigned)(m >> 32);
  const int NEG = 0xf149f2ca;
  f32x16 cc, cp;
#pragma unroll
  for (int i = 0; i < 16; ++i) {
    const int b = (i & 3) + 8 * (i >> 2);
    cc[i] = __int_as_float(__builtin_amdgcn_sbfe(mhi, b, 1) & NEG);
    cp[i] = i >= 8 ? __int_as_float(__builtin_amdgcn_sbfe(mlo, b, 1) & NEG) : 0.f;
  }
  const f32x16 sc = MFMA(kc, q, cc);
  const f32x16 sp = MFMA(kp, q, cp);
  auto finish = [&](float mx) {
    u32x4 p0, p1, pp;
#pragma unroll
    for (int i = 0; i < 4; ++i) {
      p0[i] = cvtpk(__builtin_amdgcn_exp2f(sc[2 * i] - mx), __builtin_amdgcn_exp2f(sc[2 * i + 1] - mx));
      p1[i] = cvtpk(__builtin_amdgcn_exp2f(sc[8 + 2 * i] - mx), __builtin_amdgcn_exp2f(sc[8 + 2 * i + 1] - mx));
      pp[i] = cvtpk(__builtin_amdgcn_exp2f(sp[8 + 2 * i] - mx), __builtin_amdgcn_exp2f(sp[8 + 2 * i + 1] - mx));
    }
    f32x16 o = {};
    o = MFMA(__builtin_bit_cast(bf16x8, v0), __builtin_bit_cast(bf16x8, p0), o);
    o = MFMA(__builtin_bit_cast(bf16x8, v1), __builtin_bit_cast(bf16x8, p1), o);
    o = MFMA(__builtin_bit_cast(bf16x8, vp), __builtin_bit_cast(bf16x8, pp), o);
    return o;
  };
  f32x16 o = finish(0.f);
  bool ok = o[8] >= 8.67e-19f;
#pragma unroll
  for (int i = 0; i < 9; ++i) ok = ok && (fabsf(o[i]) <= 3.0e38f);
  if (!__all(ok)) {
    float mx = fmaxf(sc[0], sc[1]);
#pragma unroll
    for (int i = 2; i < 16; i += 2) mx = fmaxf(fmaxf(mx, sc[i]), sc[i + 1]);
#pragma unroll
    for (int i = 8; i < 16; i += 2) mx = fmaxf(fmaxf(mx, sp[i]), sp[i + 1]);
    auto rr = __builtin_amdgcn_permlane32_swap(__float_as_uint(mx), __float_as_uint(mx), false, false);
    o = finish(fmaxf(__uint_as_float(rr[0]), __uint_as_float(rr[1])));
  }
  __builtin_amdgcn_raw_buffer_store_b128(pack8(o, 0, __builtin_amdgcn_rcpf(o[8])), mkrs(p.ws + WS_OB, FRAG_BYTES), (T * 32 + h) * 1024 + lane * 16, 0, 16);
}

template <bool F>
__device__ __forceinline__ void phase1(const Params& p, int wid, int lane, int br, int hq, int tb, int rot, int h2, u64 tagv, unsigned code_pf) {
  const int r32 = lane & 31, hh = lane >> 5, q4 = (lane & 15) >> 2;
  const bool main = wid < 8, halo = br && wid >= 8 && wid < 12;
  const int tt = main ? (wid & 3) + 1 : 0, half = main ? wid >> 2 : (wid >> 1) & 1;
  const int row = 32 * tt + r32, f = (row >> 1) & 7;
  const unsigned xa0 = L_XR + row * 128 + (((hh ^ (f & 1)) | (f & 6)) * 16);
  const unsigned wa0 = L_WR + (8 * hh + q4) * 128 + ((half ^ ((q4 >> 1) & 1)) * 64) + ((lane >> 4) & 1) * 32 + (lane & 3) * 8;
  f32x16 aq = {}, ak = {}, av = {};
  if (main) {
    BAR();
    WAITV(0);
    asm volatile("" :: "v"(code_pf));
    STAMP(1);
    unsigned xa[4], wa = wa0;
#pragma unroll
    for (int m = 0; m < 4; ++m) xa[m] = xa0 ^ (m << 5);
    OPAQUE(wa);
    bf16x8 xf[4], wq[4], wk[4], wv[4];
#pragma unroll
    for (int m = 0; m < 2; ++m) {
      SB(); xf[m] = lds_b128(xa[m]); wq[m] = lds_tr2(wa + m * 2048, 512);
      SB(); wk[m] = lds_tr2(wa + m * 2048 + 8192, 512);
      SB(); wv[m] = lds_tr2(wa + m * 2048 + 16384, 512);
    }
    SB();
    int slot = 0;
#pragma unroll UNR_C
    for (int u = 0; u < 8; ++u) {
      const int nslot = slot == 2 ? 0 : slot + 1;
      const int dx = (nslot - slot) * XSLOT, dw = (nslot - slot) * WSLOT;
#pragma unroll
      for (int m = 0; m < 4; ++m) {
        if (m == 2) {
          wa += dw;
#pragma unroll
          for (int i = 0; i < 4; ++i) xa[i] += dx;
        }
        const int mn = (m + 2) & 3;
        SB();
        aq = MFMA(wq[m], xf[m], aq);
        SB();
        xf[mn] = lds_b128(xa[mn]); wq[mn] = lds_tr2(wa + mn * 2048, 512);
        SB();
        ak = MFMA(wk[m], xf[m], ak);
        SB();
        wk[mn] = lds_tr2(wa + mn * 2048 + 8192, 512);
        SB();
        av = MFMA(xf[m], wv[m], av);
        SB();
        wv[mn] = lds_tr2(wa + mn * 2048 + 16384, 512);
        if (m & 1) BAR();
      }
      slot = nslot;
    }
  } else if (halo) {
    SETP(3);
    P1Loader L; L.init(p, wid - 8, lane, br, hq, tb, rot);
    L.unit(0, 0, true); L.unit(1, 1, true);
    waitv<6>();
    BAR();
    auto halo_loop = [&](auto ISV) {
      constexpr bool isv = decltype(ISV)::value;
      unsigned xa[4], wa = wa0 + (isv ? 16384 : 8192);
#pragma unroll
      for (int m = 0; m < 4; ++m) xa[m] = xa0 ^ (m << 5);
      OPAQUE(wa);
      bf16x8 xf[2], wf[2];
      f32x16 a = {};
      xf[0] = lds_b128(xa[0]); wf[0] = lds_tr2(wa, 512);
      int slot = 0;
#pragma unroll UNR_L
      for (int u = 0; u < 8; ++u) {
        const int nslot = slot == 2 ? 0 : slot + 1;
        const int dx = (nslot - slot) * XSLOT, dw = (nslot - slot) * WSLOT;
        if (u + 2 < 8) L.unit(u + 2, nslot == 2 ? 0 : nslot + 1, true);
#pragma unroll
        for (int m = 0; m < 4; ++m) {
          const int c = m & 1, n = c ^ 1;
          if (m == 3) {
            wa += dw;
#pragma unroll
            for (int i = 0; i < 4; ++i) xa[i] += dx;
          }
          const int mn = (m + 1) & 3;
          SB();
          if (isv) a = MFMA(xf[c], wf[c], a); else a = MFMA(wf[c], xf[c], a);
          SB();
          xf[n] = lds_b128(xa[mn]); wf[n] = lds_tr2(wa + mn * 2048, 512);
          if (m == 1) { if (u + 2 < 8) waitv<6>(); else waitv<0>(); }
          if (m & 1) BAR();
        }
        slot = nslot;
      }
      return a;
    };
    if (wid & 1) av = halo_loop(std::true_type{}); else ak = halo_loop(std::false_type{});
  } else {
    SETP(3);
    P1Loader L; L.init(p, wid - 8, lane, br, hq, tb, rot);
    const bool gatew = !br && wid == 8;
    if (gatew) glds16s(p.gw, lane * 16, L_GW);
    L.unit(0, 0, false); L.unit(1, 1, false);
    waitv<5>();
    BAR();
    float ga = 0.f;
    const int grow = 32 + 16 * hq + (lane >> 2), gf = (grow >> 1) & 7;
    int slot = 0;
#pragma unroll UNR_L
    for (int u = 0; u < 8; ++u) {
      const int nslot = slot == 2 ? 0 : slot + 1;
      if (u + 2 < 8) L.unit(u + 2, nslot == 2 ? 0 : nslot + 1, false);
      if (gatew) {
#pragma unroll
        for (int e = 0; e < 2; ++e) {
          const int q = 2 * (lane & 3) + e;
          const u32x4 xv = *lp<const u32x4>(L_XR + slot * XSLOT + grow * 128 + ((q ^ gf) * 16));
          const u32x4 gv = *lp<const u32x4>(L_GW + ((u + rot) & 7) * 128 + q * 16);
#pragma unroll
          for (int d = 0; d < 4; ++d) {
            const unsigned xa = xv[d], gb = gv[d];
            ga = __builtin_amdgcn_fdot2_f32_bf16(__builtin_bit_cast(bf2_t, xa), __builtin_bit_cast(bf2_t, gb), ga, false);
          }
        }
      }
      if (u + 2 < 8) waitv<5>(); else waitv<0>();
      BAR();
      BAR();
      slot = nslot;
    }
    if (gatew) {
      ga += __shfl_xor(ga, 1); ga += __shfl_xor(ga, 2);
      const float g = 1.f / (1.f + __expf(-ga));
      if ((lane & 3) == 0) __hip_atomic_store((GAS float*)(p.ws + WS_G) + tb * 128 + hq * 16 + (lane >> 2), g, RLX_AGENT);
    }
  }
  SETP(0);
  STAMP(2);
  const int h0 = 2 * half;
  GAS u64* fa1 = (GAS u64*)(p.ws + WS_FA1) + (h2 >> 1) * 64 + lane;
  if (!br) {
    if (main) {
      const int T = tb * 4 + tt - 1, hg = 4 * hq + h0;
      auto rq = mkrs(p.ws + WS_QA, FRAG_BYTES), rk = mkrs(p.ws + WS_KA, FRAG_BYTES), rv = mkrs(p.ws + WS_VA, FRAG_BYTES);
      const unsigned o0 = (hg * 64 + T) * 1024 + lane * 16;
      st16<F>(rk, o0, pack8n(ak, 0));
      st16<F>(rk, o0 + 65536, pack8n(ak, 8));
      const unsigned ov = ((hg + ((lane >> 4) & 1)) * 64 + T) * 1024 + hh * 256 + (lane & 15) * 16;
      st16<F>(rv, ov, pack8n(av, 0));
      st16<F>(rv, ov + 512, pack8n(av, 8));
      st16<F>(rq, o0, pack8(aq, 0, QSCALE));
      st16<F>(rq, o0 + 65536, pack8(aq, 8, QSCALE));
      STAMP(3);
      if (F) {
        WAITV(0);
        STAMP(5);
        if (lane == 0) set_flag(p.ws, WS_FA1 + ((hg >> 1) * 64 + T) * 8, tagv);
      }
    } else if (F && wid == 8) {
      WAITV(0);
      if (lane == 0) set_flag(p.ws, WS_FG + (tb * 8 + hq) * 8, tagv);
    }
    if (F) {
      BAR();
      STAMP(6);
      if (wid == 15) poll_flag_at(p.ws, (char*)fa1 - p.ws, lane, tagv);
      STAMP(7);
      BAR();
    }
  } else {
    if (main || (halo && !(wid & 1))) {
      LAS u32x4* kd = lp<u32x4>(L_BK + (h0 * 5 + tt) * 1024 + lane * 16);
      kd[0] = pack8n(ak, 0); kd[5 * 64] = pack8n(ak, 8);
    }
    if (main || (halo && (wid & 1))) {
      LAS u32x4* vd = lp<u32x4>(L_BV + ((h0 + ((lane >> 4) & 1)) * 5 + tt) * 1024 + hh * 256 + (lane & 15) * 16);
      vd[0] = pack8n(av, 0); vd[32] = pack8n(av, 8);
    }
    if (main) {
      LAS u32x4* qd = lp<u32x4>(L_BQ + (h0 * 4 + tt - 1) * 1024 + lane * 16);
      qd[0] = pack8(aq, 0, QSCALE); qd[4 * 64] = pack8(aq, 8, QSCALE);
    }
    STAMP(3);
    WAITL0();
    BAR();
    STAMP(5);
    local_unit(p, wid >> 2, wid & 3, 4 * hq + (wid >> 2), tb * 4 + (wid & 3), lane);
    STAMP(6);
    if (F) {
      if (wid == 3) { poll_flag_at(p.ws, (char*)fa1 - p.ws, lane, tagv); if (lane == 0) *lp<volatile unsigned>(L_MISC + 8) = 1u; }
      else spin_lds(L_MISC + 8, 1u);
      STAMP(7);
    }
  }
}

__device__ __forceinline__ void attn_tile(unsigned ka, unsigned va, int off, const bf16x8& qf, f32x16& o, const f32x16& c) {
  bf16x8 kf = lds_b128(ka + off);
  bf16x8 v0 = lds_b128(va + off);
  bf16x8 v1 = lds_b128(va + off + 512);
  f32x16 s = MFMA(kf, qf, c);
  u32x4 p0, p1;
#pragma unroll
  for (int i = 0; i < 4; ++i) p0[i] = cvtpk(__builtin_amdgcn_exp2f(s[2 * i]), __builtin_amdgcn_exp2f(s[2 * i + 1]));
#pragma unroll
  for (int i = 0; i < 4; ++i) p1[i] = cvtpk(__builtin_amdgcn_exp2f(s[8 + 2 * i]), __builtin_amdgcn_exp2f(s[8 + 2 * i + 1]));
  o = MFMA(v0, __builtin_bit_cast(bf16x8, p0), o);
  o = MFMA(v1, __builtin_bit_cast(bf16x8, p1), o);
}

template <bool F>
__device__ __forceinline__ void phase23(const Params& p, int wid, int lane, int h, int j, int tb3, int cb, u64 tag, int PH) {
  const int qt = wid & 7, kh = wid >> 3, r32 = lane & 31, hh = lane >> 5;
  LAS float* red = lp<float>(L_RED);
  const int tt = qt & 1, kq = qt >> 1;
  const int T = tb3 * 2 + tt;
  const int q4 = (lane & 15) >> 2;
  u32x4 of[8];
  float gate = 0.f;
  unsigned warm0 = 0, warm1 = 0;
  const size_t obf = (lane & 2) ? WS_FG + ((T >> 2) * 8 + 2 * (T & 3) + (lane & 1)) * 8 : WS_FB2 + ((tb3 >> 1) * 8 + kq * 2 + (lane & 1)) * 8;
  auto load_ob = [&]() {
    auto r = mkrs(p.ws + WS_OB, FRAG_BYTES);
#pragma unroll
    for (int i = 0; i < 8; ++i) of[i] = ld16<F>(r, (T * 32 + kq * 8 + i) * 1024 + lane * 16);
    gate = __hip_atomic_load((GAS float*)(p.ws + WS_G) + T * 32 + r32, RLX_AGENT);
  };
  if (PH != 3) {
    auto rq = mkrs(p.ws + WS_QA + (size_t)h * 65536, 65536), rk = mkrs(p.ws + WS_KA + (size_t)h * 65536, 65536), rv = mkrs(p.ws + WS_VA + (size_t)h * 65536, 65536);
    u32x4 pc[8];
    const int t0 = (wid & 3) + ((wid >> 2) & 1) * 32;
    pc[0] = ld16<F>(kh ? rv : rk, ((t0 + 4 * j) & 63) * 1024 + lane * 16);
    const bf16x8 qf = __builtin_bit_cast(bf16x8, ld16<F>(rq, (j * 8 + qt) * 1024 + lane * 16));
    BAR();
    {
      const unsigned v = (lane >> 2) == 0 || (lane >> 2) == 4 ? 0x3F803F80u : 0u;
      *lp<unsigned>(L_ONE + wid * 256 + lane * 4) = v;
    }
    unsigned kaddr = L_K + kh * 32768 + lane * 16, vaddr, vstep;
    if (r32 < 16) { vaddr = L_V + kh * 32768 + hh * 256 + r32 * 16; vstep = 2048; }
    else { vaddr = L_ONE + hh * 256 + ((r32 >= 20 && r32 < 28) ? 64 : 0) + ((r32 == 16 || r32 == 20) ? 0 : 16); vstep = 0; }
    const unsigned kaddr0 = kaddr, vaddr0 = vaddr;
    const unsigned pdb = ((wid & 1) ? L_V : L_K) + lane * 16;
    auto tile_of = [&](int n) { const int ti = ((n - 1) * 16 + wid) >> 1; return ti < 28 ? 4 + ti : 8 + ti; };
    auto load_rest = [&](int n0, int n1) {
#pragma unroll
      for (int n = 1; n < 8; ++n) if (n >= n0 && n < n1) pc[n] = ld16<F>((wid & 1) ? rv : rk, ((tile_of(n) + 4 * j) & 63) * 1024 + lane * 16);
    };
    auto put = [&](int n) { *lp<u32x4>(pdb + tile_of(n) * 1024) = pc[n]; };
    f32x16 o = {};
    const f32x16 zero = {};
    load_rest(1, 4);
    STAMP(9);
    *lp<u32x4>((kh ? L_V : L_K) + t0 * 1024 + lane * 16) = pc[0];
    WAITL0();
    BAR();
    STAMP(10);
    SETP(3); attn_tile(kaddr, vaddr, 0, qf, o, zero);
    SB();
    load_rest(4, 8);
    put(1); put(2);
    SB();
    SETP(2); attn_tile(kaddr, vaddr, 1024, qf, o, zero);
    put(3);
    kaddr += 2048; vaddr += vstep;
    SETP(1); attn_tile(kaddr, vaddr, 0, qf, o, zero);
    put(4); put(5);
    SETP(0); attn_tile(kaddr, vaddr, 1024, qf, o, zero);
    put(6); put(7);
    kaddr += 2048; vaddr += vstep;
    if (F) WAITV(0);
    WAITL0();
    STAMP(11);
    BAR();
    if (F && wid == 0 && lane == 0) {
      const int b = blockIdx.x, xcd = b & 7, idx = b >> 3;
      if ((idx >> 3) & 1) set_flag(p.ws, WS_FB2 + (((xcd & 1) * 8 + (idx & 7)) * 8 + (xcd >> 1) * 2 + (idx >> 4)) * 8, tag);
    }
    if (F && kh == 1) {
      const char* a0 = p.woA + (size_t)(qt * 64 + lane) * 1024 + cb * 128;
      const char* a1 = p.woB + (size_t)(qt * 64 + lane) * 1024 + cb * 128;
      asm volatile("global_load_dword %0, %2, off\n\tglobal_load_dword %1, %3, off" : "=&v"(warm0), "=&v"(warm1) : "v"(a0), "v"(a1) : "memory");
    }
    SETP(3);
    const unsigned so = (lane >> 3) * 1024 + (((lane & 7) ^ (((lane >> 4) & 1) << 2)) * 16);
    auto load_wo = [&](const char* wo, unsigned dst) {
#pragma unroll
      for (int n = 0; n < 8; ++n) glds16s(wo + (size_t)(kq * 128 + tt * 64 + n * 8) * 1024 + cb * 128, so, dst + (tt * 64 + n * 8) * 128);
    };
    vstep *= 2;
    auto quads = [&](int n) {
#pragma unroll 1
      for (int it = 0; it < n; ++it) {
        attn_tile(kaddr, vaddr, 0, qf, o, zero);
        attn_tile(kaddr, vaddr, 1024, qf, o, zero);
        attn_tile(kaddr, vaddr, 2048, qf, o, zero);
        attn_tile(kaddr, vaddr, 3072, qf, o, zero);
        kaddr += 4096; vaddr += vstep;
      }
    };
    quads(3);
    if (lane == 0) __hip_atomic_fetch_add(lp<unsigned>(L_MISC + 4), 1u, __ATOMIC_RELAXED, __HIP_MEMORY_SCOPE_WORKGROUP);
    if (F && kh == 1) {
      poll_flag_at(p.ws, obf, lane, tag);
      load_ob();
    }
    if (kh == 0) {
      const int Tq = j * 8 + qt;
      if (F) poll_flags(p.ws, WS_FG + ((Tq >> 2) * 8 + 2 * (Tq & 3)) * 8, 2, lane, tag);
      gate = __hip_atomic_load((GAS float*)(p.ws + WS_G) + Tq * 32 + r32, RLX_AGENT);
    }
    quads(1);
    if (F && kh == 1) {
      spin_lds(L_MISC + 4, 16u);
      load_wo(p.woB, l_wb(kq));
    }
    quads(1);
    SETP(2);
    quads(1);
    SETP(1);
    attn_tile(kaddr, vaddr, 0, qf, o, zero);
    attn_tile(kaddr, vaddr, 1024, qf, o, zero);
    attn_tile(kaddr, vaddr, 2048, qf, o, zero);
    SETP(0);
    attn_tile(kaddr, vaddr, 3072, qf, o, zero);
    vstep /= 2;
    STAMP(13);
    auto rO = mkrs(p.ws + WS_OA, FRAG_BYTES);
    const unsigned oo = ((j * 8 + qt) * 32 + h) * 1024 + lane * 16;
    if (kh == 1) {
#pragma unroll
      for (int i = 0; i < 9; ++i) red[(qt * 9 + i) * 64 + lane] = o[i];
      WAITV(0);
      asm volatile("" :: "v"(warm0), "v"(warm1));
    }
    WAITL0();
    BAR();
    if (kh == 0) {
      bool ok = true;
#pragma unroll
      for (int i = 0; i < 9; ++i) { o[i] += red[(qt * 9 + i) * 64 + lane]; ok = ok && (fabsf(o[i]) <= 3.0e38f); }
      ok = ok && (o[8] >= 8.67e-19f);
      if (__all(ok)) st16<F>(rO, oo, pack8(o, 0, gate * __builtin_amdgcn_rcpf(o[8])));
      else if (lane == 0) *lp<unsigned>(L_MISC) = 1u;
    }
    WAITL0();
    BAR();
    if (*lp<volatile unsigned>(L_MISC)) {
      if (F) {
        for (int n = 0; n < 8; ++n) {
          const int t = n * 8 + qt;
          *lp<u32x4>((kh ? L_V : L_K) + t * 1024 + lane * 16) = ld16<F>(kh ? rv : rk, ((t + 4 * j) & 63) * 1024 + lane * 16);
        }
        WAITL0(); BAR();
      }
      float mx = -3.0e38f;
      for (int t = 0; t < 32; ++t) {
        f32x16 s = MFMA(lds_b128(kaddr0 + t * 1024), qf, zero);
#pragma unroll
        for (int i = 0; i < 16; ++i) mx = fmaxf(mx, s[i]);
      }
      { auto rr = __builtin_amdgcn_permlane32_swap(__float_as_uint(mx), __float_as_uint(mx), false, false);
        mx = fmaxf(__uint_as_float(rr[0]), __uint_as_float(rr[1])); }
      red[wid * 64 + lane] = mx;
      WAITL0(); BAR();
      mx = fmaxf(mx, red[(wid ^ 8) * 64 + lane]);
      WAITL0(); BAR();
      f32x16 c;
#pragma unroll
      for (int i = 0; i < 16; ++i) { c[i] = -mx; o[i] = 0.f; }
      unsigned va = vaddr0;
      for (int t = 0; t < 32; ++t) { attn_tile(kaddr0 + t * 1024, va, 0, qf, o, c); va += vstep >> 1; }
      if (kh == 1) {
#pragma unroll
        for (int i = 0; i < 9; ++i) red[(qt * 9 + i) * 64 + lane] = o[i];
      }
      WAITL0(); BAR();
      if (kh == 0) {
#pragma unroll
        for (int i = 0; i < 9; ++i) o[i] += red[(qt * 9 + i) * 64 + lane];
        st16<F>(rO, oo, pack8(o, 0, gate * __builtin_amdgcn_rcpf(o[8])));
      }
      WAITL0(); BAR();
      if (F && kh == 1) { load_ob(); load_wo(p.woB, l_wb(kq)); WAITV(0); }
      BAR();
    }
    if (F && kh == 1) load_wo(p.woA, l_wa(kq));
    if (F && kh == 0) {
      WAITV(0);
      if (lane == 0) set_flag(p.ws, WS_FA2 + ((j * 8 + qt) * 32 + h) * 8, tag);
    }
    STAMP(14);
  }
  if (PH == 2) return;
  const unsigned wa = (4 * hh + q4) * 128 + ((lane >> 4) & 1) * 32 + (lane & 3) * 8;
  const unsigned sw = ((q4 >> 1) & 1) * 64;
  f32x16 a0 = {}, a1 = {};
  if (!F) {
    if (kh == 1) {
      const unsigned so = (lane >> 3) * 1024 + (((lane & 7) ^ (((lane >> 4) & 1) << 2)) * 16);
#pragma unroll
      for (int n = 0; n < 8; ++n) glds16s(p.woB + (size_t)(kq * 128 + tt * 64 + n * 8) * 1024 + cb * 128, so, l_wb(kq) + (tt * 64 + n * 8) * 128);
#pragma unroll
      for (int n = 0; n < 8; ++n) glds16s(p.woA + (size_t)(kq * 128 + tt * 64 + n * 8) * 1024 + cb * 128, so, l_wa(kq) + (tt * 64 + n * 8) * 128);
      load_ob();
      WAITV(0);
    }
    BAR();
  }
  if (kh == 1) {
    STAMP(18);
#pragma unroll
    for (int i = 0; i < 8; ++i) {
      a0 = MFMA(lds_tr2(l_wb(0) + kq * 32768 + wa + sw + i * 2048, 1024), __builtin_bit_cast(bf16x8, of[i]), a0);
      a1 = MFMA(lds_tr2(l_wb(0) + kq * 32768 + wa + (sw ^ 64) + i * 2048, 1024), __builtin_bit_cast(bf16x8, of[i]), a1);
    }
#pragma unroll
    for (int i = 0; i < 16; ++i) { a0[i] *= 1.f - gate; a1[i] *= 1.f - gate; }
    STAMP(19);
    WAITV(0);
  }
  BAR();
  using f32x4 = __attribute__((ext_vector_type(4))) float;
  auto exb = [&](int ow) { return lp<f32x4>((2 * tt + (ow >> 1)) * 32768 + (ow & 1) * 8192 + lane * 16); };
  if (kh == 1) {
    bf16x8 wf0[8], wf1[8];
#pragma unroll
    for (int i = 0; i < 4; ++i) { wf0[i] = lds_tr2(l_wa(0) + kq * 32768 + wa + sw + i * 2048, 1024); wf1[i] = lds_tr2(l_wa(0) + kq * 32768 + wa + (sw ^ 64) + i * 2048, 1024); }
    SB();
    if (F) poll_flags(p.ws, WS_FA2 + (T * 32 + kq * 8) * 8, 8, lane, tag);
    {
      auto r = mkrs(p.ws + WS_OA, FRAG_BYTES);
#pragma unroll
      for (int i = 0; i < 8; ++i) of[i] = ld16<F>(r, (T * 32 + kq * 8 + i) * 1024 + lane * 16);
    }
    STAMP(20);
    SB();
#pragma unroll
    for (int i = 0; i < 8; ++i) {
      a0 = MFMA(wf0[i], __builtin_bit_cast(bf16x8, of[i]), a0);
      a1 = MFMA(wf1[i], __builtin_bit_cast(bf16x8, of[i]), a1);
      if (i < 4) { wf0[i + 4] = lds_tr2(l_wa(0) + kq * 32768 + wa + sw + (i + 4) * 2048, 1024); wf1[i + 4] = lds_tr2(l_wa(0) + kq * 32768 + wa + (sw ^ 64) + (i + 4) * 2048, 1024); }
      SB();
    }
    STAMP(21);
#pragma unroll
    for (int ow = 0; ow < 4; ++ow) {
#pragma unroll
      for (int e = 0; e < 2; ++e) {
        const int i = 8 * (ow >> 1) + 4 * e;
        exb(ow)[(kq * 2 + e) * 64] = (ow & 1) ? f32x4{a1[i], a1[i + 1], a1[i + 2], a1[i + 3]} : f32x4{a0[i], a0[i + 1], a0[i + 2], a0[i + 3]};
      }
    }
    WAITL0();
  }
  BAR();
  if (kh == 1) {
    float s[8];
    LAS f32x4* ex = lp<f32x4>((2 * tt + (kq >> 1)) * 32768 + (kq & 1) * 8192 + lane * 16);
#pragma unroll
    for (int e = 0; e < 2; ++e) {
      const f32x4 v = (ex[(0 * 2 + e) * 64] + ex[(1 * 2 + e) * 64]) + (ex[(2 * 2 + e) * 64] + ex[(3 * 2 + e) * 64]);
      s[4 * e] = v[0]; s[4 * e + 1] = v[1]; s[4 * e + 2] = v[2]; s[4 * e + 3] = v[3];
    }
    const unsigned w0 = cvtpk(s[0], s[1]), w1 = cvtpk(s[2], s[3]), w2 = cvtpk(s[4], s[5]), w3 = cvtpk(s[6], s[7]);
    auto a = __builtin_amdgcn_permlane32_swap(w0, w2, false, false);
    auto b = __builtin_amdgcn_permlane32_swap(w1, w3, false, false);
    *(GAS u32x4*)(p.out + (size_t)(T * 32 + r32) * 1024 + cb * 128 + (kq & 1) * 64 + (kq >> 1) * 32 + hh * 16) = u32x4{a[0], b[0], a[1], b[1]};
  }
}

template <int PH>
__global__ __launch_bounds__(1024) void gated_mix_kernel(Params p) {
  const int wid = __builtin_amdgcn_readfirstlane(threadIdx.x >> 6);
  const int lane = __builtin_amdgcn_mbcnt_hi(~0u, __builtin_amdgcn_mbcnt_lo(~0u, 0u));
  const int b = blockIdx.x, xcd = b & 7, idx = b >> 3;
  unsigned code_pf = 0;
  if (PH == 0 && wid == 0 && idx < CODE_LINES / 64) {
    u64 pc;
    asm volatile("s_getpc_b64 %0" : "=s"(pc));
    const char* a = (const char*)(pc & ~127ull) + (idx * 64 + lane) * 128;
    asm volatile("global_load_dword %0, %1, off" : "=v"(code_pf) : "v"(a) : "memory");
  }
  if (wid == 15) { if (lane < 4) *lp<unsigned>(L_MISC + lane * 4) = 0u; WAITL0(); }
  if (PH >= 2) BAR();
  u64 tag = 0;
  if (PH == 0) tag = __hip_atomic_load((GAS u64*)(p.ws + WS_NONCE), RLX_AGENT) + 1;
  STAMP(0 + (PH >= 2 ? (PH - 1) * 8 : 0));
  if (PH == 0 || PH == 1) phase1<PH == 0>(p, wid, lane, (idx >> 3) & 1, (xcd >> 1) * 2 + (idx >> 4), (xcd & 1) * 8 + (idx & 7), ((idx & 7) + 2 * (idx >> 3)) & 7, xcd * 4 + (idx >> 3), tag, code_pf);
  if (PH == 0 || PH == 1) STAMP(4);
  tag = ((u64)__builtin_amdgcn_readfirstlane((unsigned)(tag >> 32)) << 32) | (unsigned)__builtin_amdgcn_readfirstlane((unsigned)tag);
  if (PH != 1) phase23<PH == 0>(p, wid, lane, xcd * 4 + (idx >> 3), idx & 7, xcd * 4 + (idx >> 3), idx & 7, tag, PH);
  STAMP(23);
  if (PH == 0 && b == 0 && wid == 8 && lane == 0) set_flag(p.ws, WS_NONCE, tag);
}

extern "C" void kernel_launch(void* const* d_in, const int* in_sizes, int n_in, void* d_out, int out_size, void* d_ws, size_t ws_size,
                              hipStream_t stream) {
  Params p;
  p.x = (const char*)d_in[0]; p.wA = (const char*)d_in[1]; p.woA = (const char*)d_in[2];
  p.wB = (const char*)d_in[4]; p.woB = (const char*)d_in[5]; p.gw = (const char*)d_in[6];
  p.out = (char*)d_out; p.ws = (char*)d_ws;
  (void)hipFuncSetAttribute((const void*)gated_mix_kernel<0>, hipFuncAttributeMaxDynamicSharedMemorySize, LDS_TOTAL);
  gated_mix_kernel<0><<<256, 1024, LDS_TOTAL, stream>>>(p);
}
```

```cpp
#define A_BARRIER
#define E_BARRIER
#include <hip/hip_runtime.h>
#include <hip/hip_bf16.h>
#include <stdint.h>
#include <type_traits>

#define UNR_C 8
#define UNR_L 8
#define FUSE 1

typedef unsigned long long u64;
using f32x16 = __attribute__((ext_vector_type(16))) float;
using bf16x8 = __attribute__((ext_vector_type(8))) short;
using s16x4 = __attribute__((ext_vector_type(4))) short;
using u32x4 = __attribute__((ext_vector_type(4))) unsigned;
using f32x4 = __attribute__((ext_vector_type(4))) float;
typedef __bf16 bf2_t __attribute__((ext_vector_type(2)));
typedef float f2_t __attribute__((ext_vector_type(2)));
#define GAS __attribute__((address_space(1)))
#define LAS __attribute__((address_space(3)))
#define MFMA(a, b, c) __builtin_amdgcn_mfma_f32_32x32x16_bf16((a), (b), (c), 0, 0, 0)
#define MFMA16(a, b, c) __builtin_amdgcn_mfma_f32_16x16x32_bf16((a), (b), (c), 0, 0, 0)
#define RLX_AGENT __ATOMIC_RELAXED, __HIP_MEMORY_SCOPE_AGENT
#define BAR() asm volatile("s_barrier" ::: "memory")
#define WAITV(n) asm volatile("s_waitcnt vmcnt(" #n ")" ::: "memory")
#define WAITL0() asm volatile("s_waitcnt lgkmcnt(0)" ::: "memory")
#define SETP(x) __builtin_amdgcn_s_setprio(x)
#define SB() __builtin_amdgcn_sched_barrier(0)
#define OPAQUE(x) asm volatile("" : "+v"(x))

constexpr size_t MB = 1u << 20;
constexpr size_t WS_NONCE = 0, WS_TMO = 64;
constexpr size_t WS_FA1 = 4096;
constexpr size_t WS_FB2 = 16384;
constexpr size_t WS_FG = 17408;
constexpr size_t WS_FA2 = 20480;
constexpr size_t WS_G = 40960;
constexpr size_t WS_QA = 1 * MB, WS_KA = 3 * MB, WS_VA = 5 * MB, WS_OA = 7 * MB, WS_OB = 9 * MB;
constexpr unsigned FRAG_BYTES = 2u << 20;
constexpr int LDS_TOTAL = 160 * 1024;
constexpr int L_XR = 0, XSLOT = 160 * 128, L_WR = 3 * XSLOT, WSLOT = 3 * 64 * 128, L_GW = L_WR + 3 * WSLOT;
constexpr int L_BK = 0, L_BV = 20480, L_BQ = 40960;
constexpr int L_K = 0, L_V = 65536, L_ONE = 131072, L_RED = L_ONE + 4096, L_MISC = L_RED + 8 * 9 * 256;
__host__ __device__ constexpr int l_wb(int kq) { return kq * 32768; }
__host__ __device__ constexpr int l_wa(int kq) { return kq * 32768 + 16384; }
static_assert(L_GW + 1024 <= LDS_TOTAL && L_MISC + 256 <= LDS_TOTAL, "lds");
constexpr float QSCALE = 0.25f * 1.4426950408889634f;
constexpr unsigned SPIN_CAP = 1u << 20;

struct Params {
  const char* x; const char* wA; const char* woA; const char* wB; const char* woB; const char* gw; char* out; char* ws;
};

__device__ __forceinline__ unsigned cvtpk(float lo, float hi) {
  f2_t v = {lo, hi};
  return __builtin_bit_cast(unsigned, __builtin_convertvector(v, bf2_t));
}
__device__ __forceinline__ u32x4 pack8(const f32x16& a, int base, float s) {
  u32x4 r;
#pragma unroll
  for (int i = 0; i < 4; ++i) r[i] = cvtpk(a[base + 2 * i] * s, a[base + 2 * i + 1] * s);
  return r;
}
__device__ __forceinline__ u32x4 pack8n(const f32x16& a, int base) {
  u32x4 r;
#pragma unroll
  for (int i = 0; i < 4; ++i) r[i] = cvtpk(a[base + 2 * i], a[base + 2 * i + 1]);
  return r;
}
template <bool F> __device__ __forceinline__ void st16(__amdgpu_buffer_rsrc_t r, unsigned off, u32x4 v) {
  __builtin_amdgcn_raw_buffer_store_b128(v, r, off, 0, F ? 16 : 0);
}
template <bool F> __device__ __forceinline__ u32x4 ld16(__amdgpu_buffer_rsrc_t r, unsigned off) {
  return __builtin_amdgcn_raw_buffer_load_b128(r, off, 0, F ? 16 : 0);
}
__device__ __forceinline__ __amdgpu_buffer_rsrc_t mkrs(const void* p, unsigned bytes) {
  return __builtin_amdgcn_make_buffer_rsrc((void*)p, 0, bytes, 0x00020000);
}
template <class T> __device__ __forceinline__ LAS T* lp(unsigned a) { return (LAS T*)(uintptr_t)a; }
__device__ __forceinline__ bf16x8 lds_b128(unsigned a) { return *lp<const bf16x8>(a); }
__device__ __forceinline__ bf16x8 lds_tr2(unsigned a, unsigned second) {
  s16x4 lo = __builtin_amdgcn_ds_read_tr16_b64_v4i16(lp<s16x4>(a));
  s16x4 hi = __builtin_amdgcn_ds_read_tr16_b64_v4i16(lp<s16x4>(a + second));
  return __builtin_shufflevector(lo, hi, 0, 1, 2, 3, 4, 5, 6, 7);
}
__device__ __forceinline__ void glds16s(const void* sbase, unsigned voff, unsigned dst) {
  unsigned sv;
  asm volatile("s_mov_b32 %0, m0\n\ts_mov_b32 m0, %3\n\ts_nop 0\n\tglobal_load_lds_dwordx4 %1, %2\n\ts_mov_b32 m0, %0" : "=&s"(sv) : "v"(voff), "s"(sbase), "s"(dst) : "memory");
}
template <int N> __device__ __forceinline__ void waitv() { asm volatile("s_waitcnt vmcnt(%0)" :: "n"(N) : "memory"); }

#define STAMP(i)

__device__ __forceinline__ void poll_flag_at(char* ws, size_t off, int lane, u64 tag) {
  GAS u64* f = (GAS u64*)(ws + off);
  unsigned spins = 0;
  while (true) {
    u64 v = __hip_atomic_load(f, RLX_AGENT);
    if (__all(v == tag)) break;
    __builtin_amdgcn_s_sleep(1);
    if (++spins > SPIN_CAP) { if (lane == 0) __hip_atomic_store((GAS unsigned*)(ws + WS_TMO), 1u, RLX_AGENT); break; }
  }
  asm volatile("" ::: "memory");
}
__device__ __forceinline__ void spin_lds(unsigned a, unsigned v) {
  while (*(LAS volatile unsigned*)(uintptr_t)a != v) __builtin_amdgcn_s_sleep(1);
  asm volatile("" ::: "memory");
}
__device__ __forceinline__ void poll_flags(char* ws, size_t off, int n, int lane, u64 tag) { poll_flag_at(ws, off + (lane < n ? lane : 0) * 8, lane, tag); }
__device__ __forceinline__ void set_flag(char* ws, size_t off, u64 tag) { __hip_atomic_store((GAS u64*)(ws + off), tag, RLX_AGENT); }

struct P1Loader {
  const char* xs; const char* wsrc; unsigned xo, wo; int lw, rot, halo_fix;
  __device__ __forceinline__ void init(const Params& p, int lw_, int lane, int br, int hq, int tb, int rot_) {
    lw = lw_; rot = rot_;
    const int qs = (lane & 7) ^ ((lane >> 4) & 3) ^ (4 * (lw & 1));
    xs = p.x + (ptrdiff_t)(tb * 128 - 32) * 1024;
    xo = (lane >> 3) * 1024 + qs * 16;
    halo_fix = tb == 0 ? 32 * 1024 : 0;
    wsrc = (br ? p.wB : p.wA) + (size_t)(8 * lw) * 3072 + hq * 128;
    wo = (lane >> 3) * 3072 + (((lane & 7) ^ (((lane >> 4) & 1) << 2) ^ ((lw & 1) << 1)) * 16);
  }
  __device__ __forceinline__ void unit(int u, int slot, bool halo) {
    const int ku = (u + rot) & 7;
    const unsigned xd = L_XR + slot * XSLOT, wd = L_WR + slot * WSLOT + lw * 1024;
    const char* xu = xs + ku * 128;
    const char* wu = wsrc + (size_t)ku * 64 * 3072;
    glds16s(xu + (4 + lw) * 8192, xo, xd + (4 + lw) * 1024);
    glds16s(wu, wo, wd);
    glds16s(xu + (12 + lw) * 8192, xo, xd + (12 + lw) * 1024);
    glds16s(wu + 1024, wo, wd + 8192);
    glds16s(wu + 2048, wo, wd + 16384);
    if (halo) glds16s(xu + lw * 8192 + halo_fix, xo, xd + lw * 1024);
  }
};

__device__ __forceinline__ void local_unit(const Params& p, int hl, int tt, int h, int T, int lane) {
  const int r32 = lane & 31, hh = lane >> 5;
  const bf16x8 q = lds_b128(L_BQ + (hl * 4 + tt) * 1024 + lane * 16);
  const bf16x8 kp = lds_b128(L_BK + (hl * 5 + tt) * 1024 + lane * 16), kc = lds_b128(L_BK + (hl * 5 + tt + 1) * 1024 + lane * 16);
  const unsigned vo = L_BV + (hl * 5 + tt) * 1024 + hh * 256 + (r32 & 15) * 16;
  u32x4 vp = *lp<const u32x4>(vo + 512), v0 = *lp<const u32x4>(vo + 1024), v1 = *lp<const u32x4>(vo + 1536);
  if (r32 >= 16) {
    const unsigned o = (r32 == 16 || r32 == 20) ? 0x3F803F80u : 0u;
    v0 = u32x4{o, o, o, o}; v1 = v0; vp = v0;
  }
  u64 m = 0x5555ull << (18 + r32);
  if (T == 0) m &= 0xFFFFFFFF00000000ull;
  m = ~(m >> (4 * hh));
  const int mlo = (int)(unsigned)m, mhi = (int)(unsigned)(m >> 32);
  const int NEG = 0xf149f2ca;
  f32x16 cc, cp;
#pragma unroll
  for (int i = 0; i < 16; ++i) {
    const int b = (i & 3) + 8 * (i >> 2);
    cc[i] = __int_as_float(__builtin_amdgcn_sbfe(mhi, b, 1) & NEG);
    cp[i] = i >= 8 ? __int_as_float(__builtin_amdgcn_sbfe(mlo, b, 1) & NEG) : 0.f;
  }
  const f32x16 sc = MFMA(kc, q, cc);
  const f32x16 sp = MFMA(kp, q, cp);
  auto finish = [&](float mx) {
    u32x4 p0, p1, pp;
#pragma unroll
    for (int i = 0; i < 4; ++i) {
      p0[i] = cvtpk(__builtin_amdgcn_exp2f(sc[2 * i] - mx), __builtin_amdgcn_exp2f(sc[2 * i + 1] - mx));
      p1[i] = cvtpk(__builtin_amdgcn_exp2f(sc[8 + 2 * i] - mx), __builtin_amdgcn_exp2f(sc[8 + 2 * i + 1] - mx));
      pp[i] = cvtpk(__builtin_amdgcn_exp2f(sp[8 + 2 * i] - mx), __builtin_amdgcn_exp2f(sp[8 + 2 * i + 1] - mx));
    }
    f32x16 o = {};
    o = MFMA(__builtin_bit_cast(bf16x8, v0), __builtin_bit_cast(bf16x8, p0), o);
    o = MFMA(__builtin_bit_cast(bf16x8, v1), __builtin_bit_cast(bf16x8, p1), o);
    o = MFMA(__builtin_bit_cast(bf16x8, vp), __builtin_bit_cast(bf16x8, pp), o);
    return o;
  };
  f32x16 o = finish(0.f);
  bool ok = o[8] >= 8.67e-19f;
#pragma unroll
  for (int i = 0; i < 9; ++i) ok = ok && (fabsf(o[i]) <= 3.0e38f);
  if (!__all(ok)) {
    float mx = fmaxf(sc[0], sc[1]);
#pragma unroll
    for (int i = 2; i < 16; i += 2) mx = fmaxf(fmaxf(mx, sc[i]), sc[i + 1]);
#pragma unroll
    for (int i = 8; i < 16; i += 2) mx = fmaxf(fmaxf(mx, sp[i]), sp[i + 1]);
    auto rr = __builtin_amdgcn_permlane32_swap(__float_as_uint(mx), __float_as_uint(mx), false, false);
    o = finish(fmaxf(__uint_as_float(rr[0]), __uint_as_float(rr[1])));
  }
  __builtin_amdgcn_raw_buffer_store_b128(pack8(o, 0, __builtin_amdgcn_rcpf(o[8])), mkrs(p.ws + WS_OB, FRAG_BYTES), (T * 32 + h) * 1024 + lane * 16, 0, 16);
}

template <bool F>
__device__ __forceinline__ void phase1(const Params& p, int wid, int lane, int br, int hq, int tb, int rot, int h2, u64 tagv) {
  const int r32 = lane & 31, hh = lane >> 5, q4 = (lane & 15) >> 2;
  const bool main = wid < 8, halo = br && wid >= 8 && wid < 12;
  const int tt = main ? (wid & 3) + 1 : 0, half = main ? wid >> 2 : (wid >> 1) & 1;
  const int row = 32 * tt + r32, f = (row >> 1) & 7;
  const unsigned xa0 = L_XR + row * 128 + (((hh ^ (f & 1)) | (f & 6)) * 16);
  const unsigned wa0 = L_WR + (8 * hh + q4) * 128 + ((half ^ ((q4 >> 1) & 1)) * 64) + ((((lane >> 4) & 1) ^ hh) * 32) + (lane & 3) * 8;
  f32x16 ak = {}, av = {};
  u32x4 fq[2], fk[2], fv[2];
  if (main) {
    BAR();
    STAMP(1);
    const int g4 = lane >> 4;
    unsigned xa[2][2], wa[2];
#pragma unroll
    for (int ta = 0; ta < 2; ++ta) {
      const int xr = 32 * tt + 16 * ta + (lane & 15), xfz = (xr >> 1) & 7;
#pragma unroll
      for (int st = 0; st < 2; ++st) xa[ta][st] = L_XR + xr * 128 + (((4 * st + g4) ^ xfz) * 16);
    }
#pragma unroll
    for (int e = 0; e < 2; ++e) wa[e] = L_WR + (8 * g4 + q4) * 128 + ((half ^ ((q4 >> 1) & 1)) * 64) + ((e ^ (g4 & 1)) * 32) + (lane & 3) * 8;
    OPAQUE(wa[0]); OPAQUE(wa[1]);
    f32x4 cq[2][2] = {}, ck[2][2] = {}, cv[2][2] = {};
    bf16x8 xf[2][2], wq[2], wk[2], wv[2];
    SB(); xf[0][0] = lds_b128(xa[0][0]); xf[0][1] = lds_b128(xa[1][0]);
    SB(); wq[0] = lds_tr2(wa[0], 512); wq[1] = lds_tr2(wa[1], 512);
    SB(); wk[0] = lds_tr2(wa[0] + 8192, 512); wk[1] = lds_tr2(wa[1] + 8192, 512);
    SB(); wv[0] = lds_tr2(wa[0] + 16384, 512); wv[1] = lds_tr2(wa[1] + 16384, 512);
    SB();
#pragma unroll
    for (int st = 0; st < 16; ++st) {
      const int c = st & 1, n = c ^ 1, nx = st + 1;
      const unsigned xo = ((nx >> 1) % 3) * XSLOT, wo = ((nx >> 1) % 3) * WSLOT + (nx & 1) * 4096;
      SB();
      cq[0][0] = MFMA16(wq[0], xf[c][0], cq[0][0]); cq[1][0] = MFMA16(wq[0], xf[c][1], cq[1][0]);
      SB();
      xf[n][0] = lds_b128(xa[0][nx & 1] + xo); xf[n][1] = lds_b128(xa[1][nx & 1] + xo);
      SB();
      cq[0][1] = MFMA16(wq[1], xf[c][0], cq[0][1]); cq[1][1] = MFMA16(wq[1], xf[c][1], cq[1][1]);
      SB();
      wq[0] = lds_tr2(wa[0] + wo, 512); wq[1] = lds_tr2(wa[1] + wo, 512);
      SB();
      ck[0][0] = MFMA16(wk[0], xf[c][0], ck[0][0]); ck[1][0] = MFMA16(wk[0], xf[c][1], ck[1][0]);
      ck[0][1] = MFMA16(wk[1], xf[c][0], ck[0][1]); ck[1][1] = MFMA16(wk[1], xf[c][1], ck[1][1]);
      SB();
      wk[0] = lds_tr2(wa[0] + wo + 8192, 512); wk[1] = lds_tr2(wa[1] + wo + 8192, 512);
      SB();
      cv[0][0] = MFMA16(xf[c][0], wv[0], cv[0][0]); cv[1][0] = MFMA16(xf[c][1], wv[0], cv[1][0]);
      cv[0][1] = MFMA16(xf[c][0], wv[1], cv[0][1]); cv[1][1] = MFMA16(xf[c][1], wv[1], cv[1][1]);
      SB();
      wv[0] = lds_tr2(wa[0] + wo + 16384, 512); wv[1] = lds_tr2(wa[1] + wo + 16384, 512);
      BAR();
    }
    auto pk2 = [](const f32x4& a, float sc, unsigned& lo, unsigned& hi) { lo = cvtpk(a[0] * sc, a[1] * sc); hi = cvtpk(a[2] * sc, a[3] * sc); };
    auto frag_t = [&](const f32x4& a, const f32x4& b, float sc) {
      unsigned a0, a1, b0, b1; pk2(a, sc, a0, a1); pk2(b, sc, b0, b1);
      auto s0 = __builtin_amdgcn_permlane16_swap(a0, b0, false, false), s1 = __builtin_amdgcn_permlane16_swap(a1, b1, false, false);
      auto t0 = __builtin_amdgcn_permlane32_swap(s0[0], s0[1], false, false), t1 = __builtin_amdgcn_permlane32_swap(s1[0], s1[1], false, false);
      return u32x4{t0[0], t1[0], t0[1], t1[1]};
    };
    auto frag_v = [&](const f32x4& a, const f32x4& b) {
      unsigned a0, a1, b0, b1; pk2(a, 1.f, a0, a1); pk2(b, 1.f, b0, b1);
      auto t0 = __builtin_amdgcn_permlane32_swap(a0, b0, false, false), t1 = __builtin_amdgcn_permlane32_swap(a1, b1, false, false);
      return u32x4{t0[0], t1[0], t0[1], t1[1]};
    };
#pragma unroll
    for (int e = 0; e < 2; ++e) { fk[e] = frag_t(ck[0][e], ck[1][e], 1.f); fv[e] = frag_v(cv[0][e], cv[1][e]); fq[e] = frag_t(cq[0][e], cq[1][e], QSCALE); }
  } else if (halo) {
    SETP(3);
    P1Loader L; L.init(p, wid - 8, lane, br, hq, tb, rot);
    L.unit(0, 0, true); L.unit(1, 1, true);
    waitv<6>();
    BAR();
    auto halo_loop = [&](auto ISV) {
      constexpr bool isv = decltype(ISV)::value;
      unsigned xa[4], wa = wa0 + (isv ? 16384 : 8192);
#pragma unroll
      for (int m = 0; m < 4; ++m) xa[m] = xa0 ^ (m << 5);
      OPAQUE(wa);
      bf16x8 xf[2], wf[2];
      f32x16 a = {};
      xf[0] = lds_b128(xa[0]); wf[0] = lds_tr2(wa, 512);
      int slot = 0;
#pragma unroll UNR_L
      for (int u = 0; u < 8; ++u) {
        const int nslot = slot == 2 ? 0 : slot + 1;
        const int dx = (nslot - slot) * XSLOT, dw = (nslot - slot) * WSLOT;
        if (u + 2 < 8) L.unit(u + 2, nslot == 2 ? 0 : nslot + 1, true);
#pragma unroll
        for (int m = 0; m < 4; ++m) {
          const int c = m & 1, n = c ^ 1;
          if (m == 3) {
            wa += dw;
#pragma unroll
            for (int i = 0; i < 4; ++i) xa[i] += dx;
          }
          const int mn = (m + 1) & 3;
          SB();
          if (isv) a = MFMA(xf[c], wf[c], a); else a = MFMA(wf[c], xf[c], a);
          SB();
          xf[n] = lds_b128(xa[mn]); wf[n] = lds_tr2(wa + mn * 2048, 512);
          if (m == 1) { if (u + 2 < 8) waitv<6>(); else waitv<0>(); }
          if (m & 1) BAR();
        }
        slot = nslot;
      }
      return a;
    };
    if (wid & 1) av = halo_loop(std::true_type{}); else ak = halo_loop(std::false_type{});
  } else {
    SETP(3);
    P1Loader L; L.init(p, wid - 8, lane, br, hq, tb, rot);
    const bool gatew = !br && wid == 8;
    if (gatew) glds16s(p.gw, lane * 16, L_GW);
    L.unit(0, 0, false); L.unit(1, 1, false);
    waitv<5>();
    BAR();
    float ga = 0.f;
    const int grow = 32 + 16 * hq + (lane >> 2), gf = (grow >> 1) & 7;
    int slot = 0;
#pragma unroll UNR_L
    for (int u = 0; u < 8; ++u) {
      const int nslot = slot == 2 ? 0 : slot + 1;
      if (u + 2 < 8) L.unit(u + 2, nslot == 2 ? 0 : nslot + 1, false);
      if (gatew) {
#pragma unroll
        for (int e = 0; e < 2; ++e) {
          const int q = 2 * (lane & 3) + e;
          const u32x4 xv = *lp<const u32x4>(L_XR + slot * XSLOT + grow * 128 + ((q ^ gf) * 16));
          const u32x4 gv = *lp<const u32x4>(L_GW + ((u + rot) & 7) * 128 + q * 16);
#pragma unroll
          for (int d = 0; d < 4; ++d) {
            const unsigned xa = xv[d], gb = gv[d];
            ga = __builtin_amdgcn_fdot2_f32_bf16(__builtin_bit_cast(bf2_t, xa), __builtin_bit_cast(bf2_t, gb), ga, false);
          }
        }
      }
      if (u + 2 < 8) waitv<5>(); else waitv<0>();
      BAR();
      BAR();
      slot = nslot;
    }
    if (gatew) {
      ga += __shfl_xor(ga, 1); ga += __shfl_xor(ga, 2);
      const float g = 1.f / (1.f + __expf(-ga));
      if ((lane & 3) == 0) __hip_atomic_store((GAS float*)(p.ws + WS_G) + tb * 128 + hq * 16 + (lane >> 2), g, RLX_AGENT);
    }
  }
  SETP(0);
  STAMP(2);
  const int h0 = 2 * half;
  GAS u64* fa1 = (GAS u64*)(p.ws + WS_FA1) + (h2 >> 1) * 64 + lane;
  if (!br) {
    if (main) {
      const int T = tb * 4 + tt - 1, hg = 4 * hq + h0;
      auto rq = mkrs(p.ws + WS_QA, FRAG_BYTES), rk = mkrs(p.ws + WS_KA, FRAG_BYTES), rv = mkrs(p.ws + WS_VA, FRAG_BYTES);
      const unsigned o0 = (hg * 64 + T) * 1024 + lane * 16;
      st16<F>(rk, o0, fk[0]);
      st16<F>(rk, o0 + 65536, fk[1]);
      st16<F>(rv, o0, fv[0]);
      st16<F>(rv, o0 + 65536, fv[1]);
      st16<F>(rq, o0, fq[0]);
      st16<F>(rq, o0 + 65536, fq[1]);
      STAMP(3);
      if (F) {
        WAITV(0);
        STAMP(5);
        if (lane == 0) set_flag(p.ws, WS_FA1 + ((hg >> 1) * 64 + T) * 8, tagv);
      }
    } else if (F && wid == 8) {
      WAITV(0);
      if (lane == 0) set_flag(p.ws, WS_FG + (tb * 8 + hq) * 8, tagv);
    }
    if (F) {
      BAR();
      STAMP(6);
      if (wid == 15) poll_flag_at(p.ws, (char*)fa1 - p.ws, lane, tagv);
      BAR();
      STAMP(7);
    }
  } else {
    if (main) {
      LAS u32x4* kd = lp<u32x4>(L_BK + (h0 * 5 + tt) * 1024 + lane * 16);
      kd[0] = fk[0]; kd[5 * 64] = fk[1];
      LAS u32x4* vd = lp<u32x4>(L_BV + (h0 * 5 + tt) * 1024 + lane * 16);
      vd[0] = fv[0]; vd[5 * 64] = fv[1];
      LAS u32x4* qd = lp<u32x4>(L_BQ + (h0 * 4 + tt - 1) * 1024 + lane * 16);
      qd[0] = fq[0]; qd[4 * 64] = fq[1];
    } else if (halo && !(wid & 1)) {
      LAS u32x4* kd = lp<u32x4>(L_BK + (h0 * 5 + tt) * 1024 + lane * 16);
      kd[0] = pack8n(ak, 0); kd[5 * 64] = pack8n(ak, 8);
    } else if (halo) {
      LAS u32x4* vd = lp<u32x4>(L_BV + ((h0 + ((lane >> 4) & 1)) * 5 + tt) * 1024 + hh * 256 + (lane & 15) * 16);
      vd[0] = pack8n(av, 0); vd[32] = pack8n(av, 8);
    }
    STAMP(3);
    WAITL0();
    BAR();
    STAMP(5);
    local_unit(p, wid >> 2, wid & 3, 4 * hq + (wid >> 2), tb * 4 + (wid & 3), lane);
    STAMP(6);
    if (F) {
      if (wid == 3) { poll_flag_at(p.ws, (char*)fa1 - p.ws, lane, tagv); if (lane == 0) *lp<volatile unsigned>(L_MISC + 8) = 1u; }
      else spin_lds(L_MISC + 8, 1u);
      STAMP(7);
    }
  }
}

__device__ __forceinline__ void attn_tile(unsigned ka, unsigned va, int off, const bf16x8& qf, f32x16& o, const f32x16& c) {
  bf16x8 kf = lds_b128(ka + off);
  bf16x8 v0 = lds_b128(va + off);
  bf16x8 v1 = lds_b128(va + off + 512);
  f32x16 s = MFMA(kf, qf, c);
  u32x4 p0, p1;
#pragma unroll
  for (int i = 0; i < 4; ++i) p0[i] = cvtpk(__builtin_amdgcn_exp2f(s[2 * i]), __builtin_amdgcn_exp2f(s[2 * i + 1]));
#pragma unroll
  for (int i = 0; i < 4; ++i) p1[i] = cvtpk(__builtin_amdgcn_exp2f(s[8 + 2 * i]), __builtin_amdgcn_exp2f(s[8 + 2 * i + 1]));
  o = MFMA(v0, __builtin_bit_cast(bf16x8, p0), o);
  o = MFMA(v1, __builtin_bit_cast(bf16x8, p1), o);
}

template <bool F>
__device__ __forceinline__ void phase23(const Params& p, int wid, int lane, int h, int j, int tb3, int cb, u64 tag, int PH) {
  const int qt = wid & 7, kh = wid >> 3, r32 = lane & 31, hh = lane >> 5;
  LAS float* red = lp<float>(L_RED);
  const int tt = qt & 1, kq = qt >> 1;
  const int T = tb3 * 2 + tt;
  const int q4 = (lane & 15) >> 2;
  u32x4 of[8];
  float gate = 0.f;
  unsigned warm0 = 0, warm1 = 0;
  const size_t obf = (lane & 2) ? WS_FG + ((T >> 2) * 8 + 2 * (T & 3) + (lane & 1)) * 8 : WS_FB2 + ((tb3 >> 1) * 8 + kq * 2 + (lane & 1)) * 8;
  auto load_ob = [&]() {
    auto r = mkrs(p.ws + WS_OB, FRAG_BYTES);
#pragma unroll
    for (int i = 0; i < 8; ++i) of[i] = ld16<F>(r, (T * 32 + kq * 8 + i) * 1024 + lane * 16);
    gate = __hip_atomic_load((GAS float*)(p.ws + WS_G) + T * 32 + r32, RLX_AGENT);
  };
  if (PH != 3) {
    auto rq = mkrs(p.ws + WS_QA + (size_t)h * 65536, 65536), rk = mkrs(p.ws + WS_KA + (size_t)h * 65536, 65536), rv = mkrs(p.ws + WS_VA + (size_t)h * 65536, 65536);
    u32x4 pc[8];
    const int t0 = (wid & 3) + ((wid >> 2) & 1) * 32;
    pc[0] = ld16<F>(kh ? rv : rk, ((t0 + 4 * j) & 63) * 1024 + lane * 16);
    const bf16x8 qf = __builtin_bit_cast(bf16x8, ld16<F>(rq, (j * 8 + qt) * 1024 + lane * 16));
    BAR();
    {
      const unsigned v = (lane >> 2) == 0 || (lane >> 2) == 4 ? 0x3F803F80u : 0u;
      *lp<unsigned>(L_ONE + wid * 256 + lane * 4) = v;
    }
    unsigned kaddr = L_K + kh * 32768 + lane * 16, vaddr, vstep;
    if (r32 < 16) { vaddr = L_V + kh * 32768 + hh * 256 + r32 * 16; vstep = 2048; }
    else { vaddr = L_ONE + hh * 256 + ((r32 >= 20 && r32 < 28) ? 64 : 0) + ((r32 == 16 || r32 == 20) ? 0 : 16); vstep = 0; }
    const unsigned kaddr0 = kaddr, vaddr0 = vaddr;
    const unsigned pdb = ((wid & 1) ? L_V : L_K) + lane * 16;
    auto tile_of = [&](int n) { const int ti = ((n - 1) * 16 + wid) >> 1; return ti < 28 ? 4 + ti : 8 + ti; };
    auto load_rest = [&](int n0, int n1) {
#pragma unroll
      for (int n = 1; n < 8; ++n) if (n >= n0 && n < n1) pc[n] = ld16<F>((wid & 1) ? rv : rk, ((tile_of(n) + 4 * j) & 63) * 1024 + lane * 16);
    };
    auto put = [&](int n) { *lp<u32x4>(pdb + tile_of(n) * 1024) = pc[n]; };
    f32x16 o = {};
    const f32x16 zero = {};
    load_rest(1, 4);
    STAMP(9);
    *lp<u32x4>((kh ? L_V : L_K) + t0 * 1024 + lane * 16) = pc[0];
    WAITL0();
    BAR();
    STAMP(10);
    SETP(3); attn_tile(kaddr, vaddr, 0, qf, o, zero);
    SB();
    load_rest(4, 8);
    put(1); put(2);
    SB();
    SETP(2); attn_tile(kaddr, vaddr, 1024, qf, o, zero);
    put(3);
    kaddr += 2048; vaddr += vstep;
    SETP(1); attn_tile(kaddr, vaddr, 0, qf, o, zero);
    put(4); put(5);
    SETP(0); attn_tile(kaddr, vaddr, 1024, qf, o, zero);
    put(6); put(7);
    kaddr += 2048; vaddr += vstep;
    if (F) WAITV(0);
    WAITL0();
    STAMP(11);
    BAR();
    if (F && wid == 0 && lane == 0) {
      const int b = blockIdx.x, xcd = b & 7, idx = b >> 3;
      if ((idx >> 3) & 1) set_flag(p.ws, WS_FB2 + (((xcd & 1) * 8 + (idx & 7)) * 8 + (xcd >> 1) * 2 + (idx >> 4)) * 8, tag);
    }
    if (F && kh == 1) {
      const char* a0 = p.woA + (size_t)(qt * 64 + lane) * 1024 + cb * 128;
      const char* a1 = p.woB + (size_t)(qt * 64 + lane) * 1024 + cb * 128;
      asm volatile("global_load_dword %0, %2, off\n\tglobal_load_dword %1, %3, off" : "=&v"(warm0), "=&v"(warm1) : "v"(a0), "v"(a1) : "memory");
    }
    SETP(3);
    const unsigned so = (lane >> 3) * 1024 + (((lane & 7) ^ (((lane >> 4) & 1) << 2)) * 16);
    auto load_wo = [&](const char* wo, unsigned dst) {
#pragma unroll
      for (int n = 0; n < 8; ++n) glds16s(wo + (size_t)(kq * 128 + tt * 64 + n * 8) * 1024 + cb * 128, so, dst + (tt * 64 + n * 8) * 128);
    };
    vstep *= 2;
    auto quads = [&](int n) {
#pragma unroll 1
      for (int it = 0; it < n; ++it) {
        attn_tile(kaddr, vaddr, 0, qf, o, zero);
        attn_tile(kaddr, vaddr, 1024, qf, o, zero);
        attn_tile(kaddr, vaddr, 2048, qf, o, zero);
        attn_tile(kaddr, vaddr, 3072, qf, o, zero);
        kaddr += 4096; vaddr += vstep;
      }
    };
    quads(3);
    if (lane == 0) __hip_atomic_fetch_add(lp<unsigned>(L_MISC + 4), 1u, __ATOMIC_RELAXED, __HIP_MEMORY_SCOPE_WORKGROUP);
    if (F && kh == 1) {
      poll_flag_at(p.ws, obf, lane, tag);
      load_ob();
    }
    if (kh == 0) {
      const int Tq = j * 8 + qt;
      if (F) poll_flags(p.ws, WS_FG + ((Tq >> 2) * 8 + 2 * (Tq & 3)) * 8, 2, lane, tag);
      gate = __hip_atomic_load((GAS float*)(p.ws + WS_G) + Tq * 32 + r32, RLX_AGENT);
    }
    quads(1);
    if (F && kh == 1) {
      spin_lds(L_MISC + 4, 16u);
      load_wo(p.woB, l_wb(kq));
    }
    quads(1);
    SETP(2);
    quads(1);
    SETP(1);
    attn_tile(kaddr, vaddr, 0, qf, o, zero);
    attn_tile(kaddr, vaddr, 1024, qf, o, zero);
    attn_tile(kaddr, vaddr, 2048, qf, o, zero);
    SETP(0);
    attn_tile(kaddr, vaddr, 3072, qf, o, zero);
    vstep /= 2;
    STAMP(13);
    auto rO = mkrs(p.ws + WS_OA, FRAG_BYTES);
    const unsigned oo = ((j * 8 + qt) * 32 + h) * 1024 + lane * 16;
    bool done = true;
    if (kh == 1) {
#pragma unroll
      for (int i = 0; i < 9; ++i) red[(qt * 9 + i) * 64 + lane] = o[i];
      WAITL0();
      if (lane == 0) *lp<volatile unsigned>(L_MISC + 16 + qt * 4) = 1u;
      WAITV(0);
      asm volatile("" :: "v"(warm0), "v"(warm1));
    }
    WAITL0();
    BAR();
    if (kh == 0) {
      bool ok = true;
#pragma unroll
      for (int i = 0; i < 9; ++i) { o[i] += red[(qt * 9 + i) * 64 + lane]; ok = ok && (fabsf(o[i]) <= 3.0e38f); }
      ok = ok && (o[8] >= 8.67e-19f);
      done = __all(ok);
      if (done) st16<F>(rO, oo, pack8(o, 0, gate * __builtin_amdgcn_rcpf(o[8])));
      else if (lane == 0) *lp<unsigned>(L_MISC) = 1u;
    }
    WAITL0();
    BAR();
    if (*lp<volatile unsigned>(L_MISC)) {
      if (F) {
        for (int n = 0; n < 8; ++n) {
          const int t = n * 8 + qt;
          *lp<u32x4>((kh ? L_V : L_K) + t * 1024 + lane * 16) = ld16<F>(kh ? rv : rk, ((t + 4 * j) & 63) * 1024 + lane * 16);
        }
        WAITL0(); BAR();
      }
      float mx = -3.0e38f;
      for (int t = 0; t < 32; ++t) {
        f32x16 s = MFMA(lds_b128(kaddr0 + t * 1024), qf, zero);
#pragma unroll
        for (int i = 0; i < 16; ++i) mx = fmaxf(mx, s[i]);
      }
      { auto rr = __builtin_amdgcn_permlane32_swap(__float_as_uint(mx), __float_as_uint(mx), false, false);
        mx = fmaxf(__uint_as_float(rr[0]), __uint_as_float(rr[1])); }
      red[wid * 64 + lane] = mx;
      WAITL0(); BAR();
      mx = fmaxf(mx, red[(wid ^ 8) * 64 + lane]);
      WAITL0(); BAR();
      f32x16 c;
#pragma unroll
      for (int i = 0; i < 16; ++i) { c[i] = -mx; o[i] = 0.f; }
      unsigned va = vaddr0;
      for (int t = 0; t < 32; ++t) { attn_tile(kaddr0 + t * 1024, va, 0, qf, o, c); va += vstep >> 1; }
      if (kh == 1) {
#pragma unroll
        for (int i = 0; i < 9; ++i) red[(qt * 9 + i) * 64 + lane] = o[i];
      }
      WAITL0(); BAR();
      if (kh == 0) {
#pragma unroll
        for (int i = 0; i < 9; ++i) o[i] += red[(qt * 9 + i) * 64 + lane];
        if (!done) st16<F>(rO, oo, pack8(o, 0, gate * __builtin_amdgcn_rcpf(o[8])));
      }
      WAITL0(); BAR();
      if (F && kh == 1) { load_ob(); load_wo(p.woB, l_wb(kq)); WAITV(0); }
      BAR();
    }
    if (F && kh == 1) load_wo(p.woA, l_wa(kq));
    if (F && kh == 0) {
      WAITV(0);
      if (lane == 0) set_flag(p.ws, WS_FA2 + ((j * 8 + qt) * 32 + h) * 8, tag);
    }
    STAMP(14);
  }
  if (PH == 2) return;
  const unsigned wa = (4 * hh + q4) * 128 + ((lane >> 4) & 1) * 32 + (lane & 3) * 8;
  const unsigned sw = ((q4 >> 1) & 1) * 64;
  f32x16 a0 = {}, a1 = {};
  if (!F) {
    if (kh == 1) {
      const unsigned so = (lane >> 3) * 1024 + (((lane & 7) ^ (((lane >> 4) & 1) << 2)) * 16);
#pragma unroll
      for (int n = 0; n < 8; ++n) glds16s(p.woB + (size_t)(kq * 128 + tt * 64 + n * 8) * 1024 + cb * 128, so, l_wb(kq) + (tt * 64 + n * 8) * 128);
#pragma unroll
      for (int n = 0; n < 8; ++n) glds16s(p.woA + (size_t)(kq * 128 + tt * 64 + n * 8) * 1024 + cb * 128, so, l_wa(kq) + (tt * 64 + n * 8) * 128);
      load_ob();
      WAITV(0);
    }
    BAR();
  }
  if (kh == 1) {
    STAMP(18);
#pragma unroll
    for (int i = 0; i < 8; ++i) {
      a0 = MFMA(lds_tr2(l_wb(0) + kq * 32768 + wa + sw + i * 2048, 1024), __builtin_bit_cast(bf16x8, of[i]), a0);
      a1 = MFMA(lds_tr2(l_wb(0) + kq * 32768 + wa + (sw ^ 64) + i * 2048, 1024), __builtin_bit_cast(bf16x8, of[i]), a1);
    }
#pragma unroll
    for (int i = 0; i < 16; ++i) { a0[i] *= 1.f - gate; a1[i] *= 1.f - gate; }
    STAMP(19);
    WAITV(0);
  }
  if (F && wid == 0 && blockIdx.x == 0) { poll_flags(p.ws, WS_FB2, 64, lane, tag); poll_flags(p.ws, WS_FB2 + 512, 64, lane, tag); }
  BAR();
  auto exb = [&](int ow) { return lp<f32x4>((2 * tt + (ow >> 1)) * 32768 + (ow & 1) * 8192 + lane * 16); };
  if (kh == 1) {
    bf16x8 wf0[8], wf1[8];
#pragma unroll
    for (int i = 0; i < 4; ++i) { wf0[i] = lds_tr2(l_wa(0) + kq * 32768 + wa + sw + i * 2048, 1024); wf1[i] = lds_tr2(l_wa(0) + kq * 32768 + wa + (sw ^ 64) + i * 2048, 1024); }
    SB();
    if (F) poll_flags(p.ws, WS_FA2 + (T * 32 + kq * 8) * 8, 8, lane, tag);
    {
      auto r = mkrs(p.ws + WS_OA, FRAG_BYTES);
#pragma unroll
      for (int i = 0; i < 8; ++i) of[i] = ld16<F>(r, (T * 32 + kq * 8 + i) * 1024 + lane * 16);
    }
    STAMP(20);
    SB();
#pragma unroll
    for (int i = 0; i < 8; ++i) {
      a0 = MFMA(wf0[i], __builtin_bit_cast(bf16x8, of[i]), a0);
      a1 = MFMA(wf1[i], __builtin_bit_cast(bf16x8, of[i]), a1);
      if (i < 4) { wf0[i + 4] = lds_tr2(l_wa(0) + kq * 32768 + wa + sw + (i + 4) * 2048, 1024); wf1[i + 4] = lds_tr2(l_wa(0) + kq * 32768 + wa + (sw ^ 64) + (i + 4) * 2048, 1024); }
      SB();
    }
    STAMP(21);
#pragma unroll
    for (int ow = 0; ow < 4; ++ow) {
#pragma unroll
      for (int e = 0; e < 2; ++e) {
        const int i = 8 * (ow >> 1) + 4 * e;
        exb(ow)[(kq * 2 + e) * 64] = (ow & 1) ? f32x4{a1[i], a1[i + 1], a1[i + 2], a1[i + 3]} : f32x4{a0[i], a0[i + 1], a0[i + 2], a0[i + 3]};
      }
    }
    WAITL0();
  }
  BAR();
  if (kh == 1) {
    float s[8];
    LAS f32x4* ex = lp<f32x4>((2 * tt + (kq >> 1)) * 32768 + (kq & 1) * 8192 + lane * 16);
#pragma unroll
    for (int e = 0; e < 2; ++e) {
      const f32x4 v = (ex[(0 * 2 + e) * 64] + ex[(1 * 2 + e) * 64]) + (ex[(2 * 2 + e) * 64] + ex[(3 * 2 + e) * 64]);
      s[4 * e] = v[0]; s[4 * e + 1] = v[1]; s[4 * e + 2] = v[2]; s[4 * e + 3] = v[3];
    }
    const unsigned w0 = cvtpk(s[0], s[1]), w1 = cvtpk(s[2], s[3]), w2 = cvtpk(s[4], s[5]), w3 = cvtpk(s[6], s[7]);
    auto a = __builtin_amdgcn_permlane32_swap(w0, w2, false, false);
    auto b = __builtin_amdgcn_permlane32_swap(w1, w3, false, false);
    *(GAS u32x4*)(p.out + (size_t)(T * 32 + r32) * 1024 + cb * 128 + (kq & 1) * 64 + (kq >> 1) * 32 + hh * 16) = u32x4{a[0], b[0], a[1], b[1]};
  }
}

template <int PH>
__global__ __launch_bounds__(1024) void gated_mix_kernel(Params p) {
  const int wid = __builtin_amdgcn_readfirstlane(threadIdx.x >> 6);
  const int lane = __builtin_amdgcn_mbcnt_hi(~0u, __builtin_amdgcn_mbcnt_lo(~0u, 0u));
  const int b = blockIdx.x, xcd = b & 7, idx = b >> 3;
  if (wid == 15) { if (lane < 16) *lp<unsigned>(L_MISC + lane * 4) = 0u; WAITL0(); }
  if (PH >= 2) BAR();
  u64 tag = 0;
  if (PH == 0) tag = __hip_atomic_load((GAS u64*)(p.ws + WS_NONCE), RLX_AGENT) + 1;
  STAMP(0 + (PH >= 2 ? (PH - 1) * 8 : 0));
  if (PH == 0 || PH == 1) phase1<PH == 0>(p, wid, lane, (idx >> 3) & 1, (xcd >> 1) * 2 + (idx >> 4), (xcd & 1) * 8 + (idx & 7), ((idx & 7) + 2 * (idx >> 3)) & 7, xcd * 4 + (idx >> 3), tag);
  if (PH == 0 || PH == 1) STAMP(4);
  tag = ((u64)__builtin_amdgcn_readfirstlane((unsigned)(tag >> 32)) << 32) | (unsigned)__builtin_amdgcn_readfirstlane((unsigned)tag);
  if (PH != 1) phase23<PH == 0>(p, wid, lane, xcd * 4 + (idx >> 3), idx & 7, xcd * 4 + (idx >> 3), idx & 7, tag, PH);
  STAMP(23);
  if (PH == 0 && b == 0 && wid == 0) {
    if (lane == 0) set_flag(p.ws, WS_NONCE, tag);
  }
}

extern "C" void kernel_launch(void* const* d_in, const int* in_sizes, int n_in, void* d_out, int out_size, void* d_ws, size_t ws_size,
                              hipStream_t stream) {
  Params p;
  p.x = (const char*)d_in[0]; p.wA = (const char*)d_in[1]; p.woA = (const char*)d_in[2];
  p.wB = (const char*)d_in[4]; p.woB = (const char*)d_in[5]; p.gw = (const char*)d_in[6];
  p.out = (char*)d_out; p.ws = (char*)d_ws;
  (void)hipFuncSetAttribute((const void*)gated_mix_kernel<0>, hipFuncAttributeMaxDynamicSharedMemorySize, LDS_TOTAL);
  gated_mix_kernel<0><<<256, 1024, LDS_TOTAL, stream>>>(p);
}
```
